```python
import torch

def _softmax(x, axis=-1):
    return torch.softmax(x, dim=axis)
def _gelu(x):
    return torch.nn.functional.gelu(x, approximate="tanh")
def _silu(x):
    return torch.nn.functional.silu(x)
def _sigmoid(x):
    return torch.sigmoid(x)
def _layernorm(x, axis=-1, eps=1e-5):
    m = x.mean(dim=axis, keepdim=True)
    v = x.var(dim=axis, keepdim=True, unbiased=False)
    return (x - m) / torch.sqrt(v + eps)
def _scatter_add(tgt, idx, src, axis=0):
    return tgt.clone().index_add_(axis, idx, src)
def _segment_sum(src, seg, n):
    out = torch.zeros((n,) + src.shape[1:], dtype=src.dtype)
    return out.index_add_(0, seg, src)
def _pad(x, pw):
    flat = tuple(v for pair in reversed(pw) for v in pair)
    return torch.nn.functional.pad(x, flat)

kernel_name = 'gated_mix__sliding_block_cached__gqa_block_cached__decode'

def setup_inputs():

    _w = (lambda a: (lambda t: t.to(torch.bfloat16) if t.is_floating_point() else t)(torch.as_tensor(a)))
    def _A_setup():
        B, Sq, D = 1, 1, 448
        Hq, Hkv, Sk, Dh = 16, 8, 8192, 256
        assert Hq % Hkv == 0, (Hq, Hkv)
        return {
            "x": _w((lambda *s: torch.randn(*s, dtype=torch.bfloat16) * 0.05)(B, Sq, D)),
            "k_cache": _w((lambda *s: torch.randn(*s, dtype=torch.bfloat16) * 3.2)(B, Hkv, Sk, Dh)),
            "v_cache": _w((lambda *s: torch.randn(*s, dtype=torch.bfloat16) * 0.05)(B, Hkv, Sk, Dh)),
            "wq": _w((lambda *s: torch.randn(*s, dtype=torch.bfloat16) * 3.2)(D, Hq * Dh) * (D ** -0.5)),
            "wo": _w((lambda *s: torch.randn(*s, dtype=torch.bfloat16) * 0.05)(Hq * Dh, D) * ((Hq * Dh) ** -0.5)),
        }
    _A_in = _A_setup()

    def _B_setup():
        B, Sq, D = 1, 1, 448
        Hq, Hkv, Sk, Dh = 16, 8, 8192, 256
        assert Hq % Hkv == 0, (Hq, Hkv)
        return {
            "x": _w((lambda *s: torch.randn(*s, dtype=torch.bfloat16) * 0.05)(B, Sq, D)),
            "k_cache": _w((lambda *s: torch.randn(*s, dtype=torch.bfloat16) * 3.2)(B, Hkv, Sk, Dh)),
            "v_cache": _w((lambda *s: torch.randn(*s, dtype=torch.bfloat16) * 0.05)(B, Hkv, Sk, Dh)),
            "wq": _w((lambda *s: torch.randn(*s, dtype=torch.bfloat16) * 3.2)(D, Hq * Dh) * (D ** -0.5)),
            "wo": _w((lambda *s: torch.randn(*s, dtype=torch.bfloat16) * 0.05)(Hq * Dh, D) * ((Hq * Dh) ** -0.5)),
        }
    _B_in = _B_setup()

    return {
        "A_x": _A_in["x"],
        "A_k_cache": _A_in["k_cache"],
        "A_v_cache": _A_in["v_cache"],
        "A_wq": _A_in["wq"],
        "A_wo": _A_in["wo"],
        "B_x": _B_in["x"],
        "B_k_cache": _B_in["k_cache"],
        "B_v_cache": _B_in["v_cache"],
        "B_wq": _B_in["wq"],
        "B_wo": _B_in["wo"],
        "gate_w": _w((lambda *s: torch.randn(*s, dtype=torch.bfloat16) * 0.1)(448) * 16.0),
    }

def reference(A_x, A_k_cache, A_v_cache, A_wq, A_wo, B_x, B_k_cache, B_v_cache, B_wq, B_wo, gate_w):
    def _A_ref(x, k_cache, v_cache, wq, wo):
        B, Sq, D = x.shape
        Hq, Dh = 16, 256
        Hkv, Sk = k_cache.shape[1], k_cache.shape[2]
        W = min(4096, max(256, Sk // 4))
        rep = Hq // Hkv
        q = (lambda x, *d: x.permute(*d))((x @ wq).reshape(B, Sq, Hq, Dh), 0, 2, 1, 3)
        k = (lambda x, n, axis: torch.repeat_interleave(x, n, dim=axis))(k_cache[:, :, Sk - W:, :], rep, axis=1)
        v = (lambda x, n, axis: torch.repeat_interleave(x, n, dim=axis))(v_cache[:, :, Sk - W:, :], rep, axis=1)
        scores = torch.einsum("bhqd,bhkd->bhqk", q, k) * (Dh ** -0.5)
        p = _softmax(scores, axis=-1)
        o = torch.einsum("bhqk,bhkd->bhqd", p, v)
        return (lambda x, *d: x.permute(*d))(o, 0, 2, 1, 3).reshape(B, Sq, Hq * Dh) @ wo

    def _B_ref(x, k_cache, v_cache, wq, wo):
        B, Sq, D = x.shape
        Hq, Dh = 16, 256
        Hkv = k_cache.shape[1]
        rep = Hq // Hkv
        q = (lambda x, *d: x.permute(*d))((x @ wq).reshape(B, Sq, Hq, Dh), 0, 2, 1, 3)
        k = (lambda x, n, axis: torch.repeat_interleave(x, n, dim=axis))(k_cache, rep, axis=1)
        v = (lambda x, n, axis: torch.repeat_interleave(x, n, dim=axis))(v_cache, rep, axis=1)
        scores = torch.einsum("bhqd,bhkd->bhqk", q, k) * (Dh ** -0.5)
        p = _softmax(scores, axis=-1)
        o = torch.einsum("bhqk,bhkd->bhqd", p, v)
        return (lambda x, *d: x.permute(*d))(o, 0, 2, 1, 3).reshape(B, Sq, Hq * Dh) @ wo

    x = A_x
    g = _sigmoid((x * gate_w).sum(-1, keepdims=True))
    a = _A_ref(x, A_k_cache, A_v_cache, A_wq, A_wo)
    b = _B_ref(x, B_k_cache, B_v_cache, B_wq, B_wo)
    return g * a + (1 - g) * b
```

```cpp
#include <hip/hip_runtime.h>
#include <hip/hip_bf16.h>
#include <stdint.h>

namespace {
constexpr int DM = 448, HQ = 16, HKV = 8, SK = 8192, DH = 256;
constexpr int WIN = 2048;
constexpr int NSA = 7, NSB = 25;
constexpr int NWG = 256;

typedef unsigned int u32;
typedef __attribute__((ext_vector_type(4))) unsigned u32x4;

__device__ __forceinline__ float bf2f(unsigned short h) { return __uint_as_float(((u32)h) << 16); }
__device__ __forceinline__ float bflo(u32 w) { return __uint_as_float(w << 16); }
__device__ __forceinline__ float bfhi(u32 w) { return __uint_as_float(w & 0xffff0000u); }
__device__ __forceinline__ unsigned short f2bf(float f) {
  u32 u = __float_as_uint(f);
  if ((u & 0x7fffffffu) > 0x7f800000u) return (unsigned short)((u >> 16) | 0x40);
  return (unsigned short)((u + 0x7fffu + ((u >> 16) & 1u)) >> 16);
}
__device__ __forceinline__ float rbf(float f) { return bf2f(f2bf(f)); }

constexpr size_t WS_Q = 0;
constexpr size_t WS_PML = 65536;
constexpr size_t WS_PO = WS_PML + 256 * 4 * 4;
constexpr size_t WS_OUTP = WS_PO + 256 * 2 * 256 * 4;

__global__ void __launch_bounds__(256) k_qproj(const unsigned short* __restrict__ x, const unsigned short* __restrict__ wqA,
                                               const unsigned short* __restrict__ wqB, unsigned char* ws) {
  __shared__ float red[64][33];
  const int j = blockIdx.x, t = threadIdx.x;
  const int mod = j >> 7, col0 = (j & 127) * 32;
  const unsigned short* wq = mod ? wqB : wqA;
  const int seg = t & 3, r = t >> 2;
  float acc[8];
#pragma unroll
  for (int k = 0; k < 8; ++k) acc[k] = 0.f;
  for (int i = 0; i < 7; ++i) {
    const int row = r + 64 * i;
    const u32x4 w = *reinterpret_cast<const u32x4*>(wq + (size_t)row * 4096 + col0 + seg * 8);
    const float xv = bf2f(x[row]);
#pragma unroll
    for (int k = 0; k < 4; ++k) { acc[2 * k] = fmaf(xv, bflo(w[k]), acc[2 * k]); acc[2 * k + 1] = fmaf(xv, bfhi(w[k]), acc[2 * k + 1]); }
  }
#pragma unroll
  for (int k = 0; k < 8; ++k) red[r][seg * 8 + k] = acc[k];
  __syncthreads();
  if (t < 32) {
    float s = 0.f;
    for (int i = 0; i < 64; ++i) s += red[i][t];
    reinterpret_cast<unsigned short*>(ws + WS_Q)[mod * 4096 + col0 + t] = f2bf(s);
  }
}

__device__ __forceinline__ void wg_map(int j, int& mod, int& h, int& s, int& k0, int& k1) {
  if (j < 8 * NSA) { mod = 0; h = j / NSA; s = j % NSA;
    const int per = ((WIN + NSA - 1) / NSA + 1) & ~1;
    k0 = s * per; k1 = min(k0 + per, WIN); k0 += SK - WIN; k1 += SK - WIN;
  } else { const int jj = j - 8 * NSA; mod = 1; h = jj / NSB; s = jj % NSB;
    const int per = ((SK + NSB - 1) / NSB + 1) & ~1;
    k0 = s * per; k1 = min(k0 + per, SK);
  }
}

__global__ void __launch_bounds__(256) k_attn(const unsigned short* __restrict__ kA, const unsigned short* __restrict__ vA,
                                              const unsigned short* __restrict__ kB, const unsigned short* __restrict__ vB, unsigned char* ws) {
  __shared__ float sm[4][2][2];
  __shared__ float so[4][2][256];
  const int j = blockIdx.x, t = threadIdx.x, lane = t & 63, wave = t >> 6;
  int mod, h, s, k0, k1; wg_map(j, mod, h, s, k0, k1);
  const unsigned short* kc = (mod ? kB : kA) + (size_t)h * SK * DH;
  const unsigned short* vc = (mod ? vB : vA) + (size_t)h * SK * DH;
  const unsigned short* q = reinterpret_cast<const unsigned short*>(ws + WS_Q) + mod * 4096 + h * 512;
  const int half = lane >> 5, c = lane & 31;
  float qf[2][8];
#pragma unroll
  for (int g = 0; g < 2; ++g)
#pragma unroll
    for (int k = 0; k < 8; ++k) qf[g][k] = bf2f(q[g * 256 + c * 8 + k]);
  float m[2] = {-INFINITY, -INFINITY}, l[2] = {0.f, 0.f}, acc[2][8];
#pragma unroll
  for (int g = 0; g < 2; ++g)
#pragma unroll
    for (int k = 0; k < 8; ++k) acc[g][k] = 0.f;
  for (int kp = k0 + 2 * wave; kp < k1; kp += 8) {
    const int key = kp + half;
    const u32x4 kv = *reinterpret_cast<const u32x4*>(kc + (size_t)key * DH + c * 8);
    const u32x4 vv = *reinterpret_cast<const u32x4*>(vc + (size_t)key * DH + c * 8);
    float sc[2];
#pragma unroll
    for (int g = 0; g < 2; ++g) {
      float d = 0.f;
#pragma unroll
      for (int k = 0; k < 4; ++k) { d = fmaf(qf[g][2 * k], bflo(kv[k]), d); d = fmaf(qf[g][2 * k + 1], bfhi(kv[k]), d); }
#pragma unroll
      for (int o = 16; o >= 1; o >>= 1) d += __shfl_xor(d, o, 64);
      sc[g] = rbf(d) * 0.0625f;
    }
#pragma unroll
    for (int g = 0; g < 2; ++g) {
      const float mn = fmaxf(m[g], sc[g]);
      const float al = __expf(m[g] - mn), p = __expf(sc[g] - mn);
      l[g] = l[g] * al + p; m[g] = mn;
#pragma unroll
      for (int k = 0; k < 4; ++k) {
        acc[g][2 * k] = acc[g][2 * k] * al + p * bflo(vv[k]);
        acc[g][2 * k + 1] = acc[g][2 * k + 1] * al + p * bfhi(vv[k]);
      }
    }
  }
#pragma unroll
  for (int g = 0; g < 2; ++g) {
    const float mo = __shfl_xor(m[g], 32, 64), lo = __shfl_xor(l[g], 32, 64);
    const float mn = fmaxf(m[g], mo);
    const float a0 = (m[g] == -INFINITY) ? 0.f : __expf(m[g] - mn), a1 = (mo == -INFINITY) ? 0.f : __expf(mo - mn);
    l[g] = l[g] * a0 + lo * a1;
#pragma unroll
    for (int k = 0; k < 8; ++k) { const float ao = __shfl_xor(acc[g][k], 32, 64); acc[g][k] = acc[g][k] * a0 + ao * a1; }
    m[g] = mn;
    if (lane == 0) { sm[wave][g][0] = m[g]; sm[wave][g][1] = l[g]; }
    if (half == 0) {
#pragma unroll
      for (int k = 0; k < 8; ++k) so[wave][g][c * 8 + k] = acc[g][k];
    }
  }
  __syncthreads();
  float* pml = reinterpret_cast<float*>(ws + WS_PML) + (size_t)j * 4;
  float* po = reinterpret_cast<float*>(ws + WS_PO) + (size_t)j * 512;
  for (int idx = t; idx < 512; idx += 256) {
    const int g = idx >> 8, d = idx & 255;
    float M = -INFINITY;
    for (int w = 0; w < 4; ++w) M = fmaxf(M, sm[w][g][0]);
    float L = 0.f, o = 0.f;
    for (int w = 0; w < 4; ++w) { const float wt = (sm[w][g][0] == -INFINITY) ? 0.f : __expf(sm[w][g][0] - M); L += wt * sm[w][g][1]; o += wt * so[w][g][d]; }
    po[idx] = o;
    if (d == 0) { pml[g * 2] = M; pml[g * 2 + 1] = L; }
  }
}

__global__ void __launch_bounds__(256) k_proj(const unsigned short* __restrict__ woA, const unsigned short* __restrict__ woB, unsigned char* ws) {
  __shared__ float so[32];
  __shared__ float red[4][448];
  const int j = blockIdx.x, t = threadIdx.x;
  const int mod = j >> 7, row0 = (j & 127) * 32, hq = row0 >> 8, h = hq >> 1, g = hq & 1, d0 = row0 & 255;
  const int ns = mod ? NSB : NSA, wg0 = mod ? 8 * NSA + h * NSB : h * NSA;
  const float* pml = reinterpret_cast<const float*>(ws + WS_PML);
  const float* po = reinterpret_cast<const float*>(ws + WS_PO);
  if (t < 32) {
    float M = -INFINITY;
    for (int s = 0; s < ns; ++s) M = fmaxf(M, pml[(size_t)(wg0 + s) * 4 + g * 2]);
    float L = 0.f, o = 0.f;
    for (int s = 0; s < ns; ++s) {
      const float ms = pml[(size_t)(wg0 + s) * 4 + g * 2], ls = pml[(size_t)(wg0 + s) * 4 + g * 2 + 1];
      const float wt = (ms == -INFINITY) ? 0.f : __expf(ms - M);
      L += wt * ls; o += wt * po[(size_t)(wg0 + s) * 512 + g * 256 + d0 + t];
    }
    so[t] = rbf(o / L);
  }
  __syncthreads();
  const unsigned short* wo = (mod ? woB : woA) + (size_t)row0 * DM;
  if (t < 224) {
    const int rg = t / 56, cg = t % 56;
    float acc[8];
#pragma unroll
    for (int k = 0; k < 8; ++k) acc[k] = 0.f;
    for (int r = rg; r < 32; r += 4) {
      const u32x4 w = *reinterpret_cast<const u32x4*>(wo + (size_t)r * DM + cg * 8);
      const float ov = so[r];
#pragma unroll
      for (int k = 0; k < 4; ++k) { acc[2 * k] = fmaf(ov, bflo(w[k]), acc[2 * k]); acc[2 * k + 1] = fmaf(ov, bfhi(w[k]), acc[2 * k + 1]); }
    }
#pragma unroll
    for (int k = 0; k < 8; ++k) red[rg][cg * 8 + k] = acc[k];
  }
  __syncthreads();
  float* outp = reinterpret_cast<float*>(ws + WS_OUTP) + (size_t)j * DM;
  for (int c = t; c < DM; c += 256) outp[c] = (red[0][c] + red[1][c]) + (red[2][c] + red[3][c]);
}

__global__ void __launch_bounds__(448) k_final(const unsigned short* __restrict__ x, const unsigned short* __restrict__ gw, unsigned char* ws, unsigned short* out) {
  __shared__ float sred[448];
  const int t = threadIdx.x;
  sred[t] = rbf(bf2f(x[t]) * bf2f(gw[t]));
  __syncthreads();
  float gs = 0.f;
  for (int i = 0; i < DM; ++i) gs += sred[i];
  gs = rbf(gs);
  const float g = rbf(1.f / (1.f + __expf(-gs)));
  const float* outp = reinterpret_cast<const float*>(ws + WS_OUTP);
  float a = 0.f, b = 0.f;
  for (int j = 0; j < 128; ++j) a += outp[(size_t)j * DM + t];
  for (int j = 128; j < 256; ++j) b += outp[(size_t)j * DM + t];
  a = rbf(a); b = rbf(b);
  const float u = rbf(g * a), w = rbf(rbf(1.f - g) * b);
  out[t] = f2bf(u + w);
}
}

extern "C" void kernel_launch(void* const* d_in, const int* in_sizes, int n_in, void* d_out, int out_size, void* d_ws, size_t ws_size,
                              hipStream_t stream) {
  const unsigned short* x = (const unsigned short*)d_in[0];
  const unsigned short* kA = (const unsigned short*)d_in[1];
  const unsigned short* vA = (const unsigned short*)d_in[2];
  const unsigned short* wqA = (const unsigned short*)d_in[3];
  const unsigned short* woA = (const unsigned short*)d_in[4];
  const unsigned short* kB = (const unsigned short*)d_in[6];
  const unsigned short* vB = (const unsigned short*)d_in[7];
  const unsigned short* wqB = (const unsigned short*)d_in[8];
  const unsigned short* woB = (const unsigned short*)d_in[9];
  const unsigned short* gw = (const unsigned short*)d_in[10];
  unsigned char* ws = (unsigned char*)d_ws;
  k_qproj<<<256, 256, 0, stream>>>(x, wqA, wqB, ws);
  k_attn<<<256, 256, 0, stream>>>(kA, vA, kB, vB, ws);
  k_proj<<<256, 256, 0, stream>>>(woA, woB, ws);
  k_final<<<1, 448, 0, stream>>>(x, gw, ws, (unsigned short*)d_out);
}
```

```cpp
#include <hip/hip_runtime.h>
#include <hip/hip_bf16.h>
#include <stdint.h>

namespace {
typedef unsigned short u16;
typedef unsigned int u32;
typedef unsigned long long u64;
typedef __attribute__((ext_vector_type(4))) unsigned u32x4;
typedef __attribute__((ext_vector_type(2))) unsigned u32x2;
typedef __attribute__((ext_vector_type(4))) float f32x4;
typedef __attribute__((ext_vector_type(2))) float f32x2;
typedef __attribute__((ext_vector_type(2))) __bf16 bf16x2;

constexpr int DM = 448, SK = 8192, DH = 256, WINDOW = 2048;
constexpr int NSA = 7, NSB = 25;
constexpr int PERA = 294, PERB = 328;
constexpr int NTHR = 512, NWAVE = 8;
constexpr int NSLOT = 21;
#define EARLY_K 6
constexpr int EARLY = EARLY_K;

constexpr u32 WS_EPOCH = 0;
constexpr u32 WS_FQ = 0x1000;
constexpr u32 WS_F1 = 0x8000;
constexpr u32 WS_F2 = 0xC000;
constexpr u32 WS_Q = 0x10000;
constexpr u32 WS_O = 0x20000;
constexpr u32 WS_R = 0x200000;
constexpr u32 WS_BYTES = 0x300000;
constexpr int AUX_SC1 = 16;
constexpr int AUX_NT = 2;
constexpr unsigned SPIN_CAP = 1u << 17;

constexpr int L_RED1 = 0;
constexpr int L_GP = 256;
constexpr int L_Q = 288;
constexpr int L_P = 544;
constexpr int L_ML = 1312;
constexpr int L_MRG = 1344;
constexpr int L_RED2 = L_MRG;
constexpr int L_RED3 = L_MRG + 256;
constexpr int L_FIN = L_MRG + 3840;
constexpr int L_TOTAL = L_MRG + 4096;

__device__ __forceinline__ float bf2f(u16 h) { return __uint_as_float(((u32)h) << 16); }
__device__ __forceinline__ float bflo(u32 w) { return __uint_as_float(w << 16); }
__device__ __forceinline__ float bfhi(u32 w) { return __uint_as_float(w & 0xffff0000u); }
__device__ __forceinline__ u32 f2bf_bits(float f) {
  return (u32)__builtin_bit_cast(u16, (__bf16)f);
}
__device__ __forceinline__ float rbf(float f) { return __uint_as_float(f2bf_bits(f) << 16); }
__device__ __forceinline__ u32 pack2(float lo, float hi) { return f2bf_bits(lo) | (f2bf_bits(hi) << 16); }
__device__ __forceinline__ float dot2(u32 a, u32 b, float c) {
  return __builtin_amdgcn_fdot2_f32_bf16(__builtin_bit_cast(bf16x2, a), __builtin_bit_cast(bf16x2, b), c, false);
}
template <int CTRL> __device__ __forceinline__ float dpp(float x) {
  return __builtin_bit_cast(float, __builtin_amdgcn_mov_dpp(__builtin_bit_cast(int, x), CTRL, 0xf, 0xf, true));
}
constexpr int XOR1 = 0xB1, XOR2 = 0x4E, XOR7 = 0x141, XOR8 = 0x128, ROR4 = 0x124;
__device__ __forceinline__ float swap16_add(float a, float b) {
  auto s = __builtin_amdgcn_permlane16_swap(__float_as_uint(a), __float_as_uint(b), false, false);
  return __uint_as_float(s[0]) + __uint_as_float(s[1]);
}
__device__ __forceinline__ float swap32_add(float a, float b) {
  auto s = __builtin_amdgcn_permlane32_swap(__float_as_uint(a), __float_as_uint(b), false, false);
  return __uint_as_float(s[0]) + __uint_as_float(s[1]);
}
__device__ __forceinline__ float x16_max(float x) {
  auto s = __builtin_amdgcn_permlane16_swap(__float_as_uint(x), __float_as_uint(x), false, false);
  return fmaxf(__uint_as_float(s[0]), __uint_as_float(s[1]));
}
__device__ __forceinline__ float x32_max(float x) {
  auto s = __builtin_amdgcn_permlane32_swap(__float_as_uint(x), __float_as_uint(x), false, false);
  return fmaxf(__uint_as_float(s[0]), __uint_as_float(s[1]));
}
__device__ __forceinline__ void lds_barrier() { asm volatile("s_waitcnt lgkmcnt(0)\n\ts_barrier" ::: "memory"); }
__device__ __forceinline__ void lds_wait() { asm volatile("s_waitcnt lgkmcnt(0)" ::: "memory"); }
__device__ __forceinline__ void drain_vm() { asm volatile("s_waitcnt vmcnt(0)" ::: "memory"); }
__device__ __forceinline__ void flag_store(unsigned char* ws, u32 off, u64 tag) {
  __hip_atomic_store(reinterpret_cast<u64*>(ws + off), tag, __ATOMIC_RELAXED, __HIP_MEMORY_SCOPE_AGENT);
}
__device__ __forceinline__ u64 flag_load(unsigned char* ws, u32 off) {
  return __hip_atomic_load(reinterpret_cast<u64*>(ws + off), __ATOMIC_RELAXED, __HIP_MEMORY_SCOPE_AGENT);
}
__device__ __forceinline__ void wait_flags(unsigned char* ws, u32 off, bool mine, u64 tag) {
  for (unsigned spins = 0; spins < SPIN_CAP; ++spins) {
    const bool ok = !mine || flag_load(ws, off) == tag;
    if (__all(ok)) break;
    __builtin_amdgcn_s_sleep(2);
  }
}
__device__ __forceinline__ void acquire_agent() {
  __builtin_amdgcn_fence(__ATOMIC_ACQUIRE, "agent");
  asm volatile("s_waitcnt vmcnt(0)" ::: "memory");
}

struct Ptrs {
  const u16* x; const u16* kA; const u16* vA; const u16* wqA; const u16* woA;
  const u16* kB; const u16* vB; const u16* wqB; const u16* woB; const u16* gw;
  u16* out; unsigned char* ws;
};

__global__ void __launch_bounds__(NTHR, 2) fused_decode(Ptrs P) {
  __shared__ __attribute__((aligned(16))) float lds[L_TOTAL];
  const int j = blockIdx.x, t = threadIdx.x, lane = t & 63;
  const int wave = __builtin_amdgcn_readfirstlane(t >> 6);
  unsigned char* ws = P.ws;
  const u64 tag = *reinterpret_cast<const u64*>(ws + WS_EPOCH) + 1ull;
  const __amdgpu_buffer_rsrc_t rsW = __builtin_amdgcn_make_buffer_rsrc(ws, 0, (int)WS_BYTES, 0x00020000);

  int mod, h, s, k0, k1;
  if (j < 8 * NSA) { mod = 0; h = j / NSA; s = j - h * NSA; k0 = s * PERA; k1 = min(k0 + PERA, WINDOW); k0 += SK - WINDOW; k1 += SK - WINDOW; }
  else { const int jj = j - 8 * NSA; mod = 1; h = jj / NSB; s = jj - h * NSB; k0 = s * PERB; k1 = min(k0 + PERB, SK); }
  const int np = (k1 - k0) >> 1;
  const u16* kbase = (mod ? P.kB : P.kA) + (size_t)h * SK * DH;
  const u16* vbase = (mod ? P.vB : P.vA) + (size_t)h * SK * DH;
  const __amdgpu_buffer_rsrc_t rsK = __builtin_amdgcn_make_buffer_rsrc((void*)kbase, 0, SK * DH * 2, 0x00020000);
  const __amdgpu_buffer_rsrc_t rsV = __builtin_amdgcn_make_buffer_rsrc((void*)vbase, 0, SK * DH * 2, 0x00020000);
  const int voff = (lane >> 5) * 512 + (lane & 31) * 16;
#define KV_SOFF(i) ((k0 + 2 * min(wave + 8 * (i), np - 1)) * 512)

  const int mod1 = j >> 7, col0 = (j & 127) * 32;
  const u16* wq = mod1 ? P.wqB : P.wqA;
  const int seg = t & 3, r0 = t >> 2;
  u32x4 wv[4]; u16 xr[4];
#pragma unroll
  for (int i = 0; i < 4; ++i) {
    const int row = min(r0 + 128 * i, DM - 1);
    wv[i] = __builtin_nontemporal_load(reinterpret_cast<const u32x4*>(wq + (size_t)row * 4096 + col0 + seg * 8));
    xr[i] = P.x[row];
  }
  const int tg = min(t, DM - 1);
  const u16 gxr = P.x[tg], gwr = P.gw[tg];

  u32x4 Kr[NSLOT], Vr[NSLOT];
#pragma unroll
  for (int i = 0; i < EARLY; ++i) Kr[i] = __builtin_amdgcn_raw_buffer_load_b128(rsK, voff, KV_SOFF(i), AUX_NT);
  __builtin_amdgcn_sched_barrier(0);

  const float gp = (t < DM) ? rbf(bf2f(gxr) * bf2f(gwr)) : 0.f;
  float xv[4];
#pragma unroll
  for (int i = 0; i < 4; ++i) xv[i] = (r0 + 128 * i < DM) ? bf2f(xr[i]) : 0.f;
  {
    float acc[8];
#pragma unroll
    for (int k = 0; k < 8; ++k) acc[k] = 0.f;
#pragma unroll
    for (int i = 0; i < 4; ++i)
#pragma unroll
      for (int k = 0; k < 4; ++k) { acc[2 * k] = fmaf(xv[i], bflo(wv[i][k]), acc[2 * k]); acc[2 * k + 1] = fmaf(xv[i], bfhi(wv[i][k]), acc[2 * k + 1]); }
#pragma unroll
    for (int k = 0; k < 8; ++k) {
      float v = acc[k];
      v += dpp<ROR4>(v); v += dpp<XOR8>(v);
      v = swap16_add(v, v); v = swap32_add(v, v);
      acc[k] = v;
    }
    if (lane < 4) {
      *reinterpret_cast<f32x4*>(&lds[L_RED1 + wave * 32 + lane * 8]) = f32x4{acc[0], acc[1], acc[2], acc[3]};
      *reinterpret_cast<f32x4*>(&lds[L_RED1 + wave * 32 + lane * 8 + 4]) = f32x4{acc[4], acc[5], acc[6], acc[7]};
    }
    float g = gp;
    g += dpp<XOR1>(g); g += dpp<XOR2>(g); g += dpp<ROR4>(g); g += dpp<XOR8>(g);
    g = swap16_add(g, g); g = swap32_add(g, g);
    if (lane == 0) lds[L_GP + wave] = g;
  }
  lds_barrier();
  if (wave == 0) {
    if (lane < 4) {
      f32x4 a = *reinterpret_cast<const f32x4*>(&lds[L_RED1 + lane * 8]);
      f32x4 b = *reinterpret_cast<const f32x4*>(&lds[L_RED1 + lane * 8 + 4]);
#pragma unroll
      for (int w = 1; w < NWAVE; ++w) { a += *reinterpret_cast<const f32x4*>(&lds[L_RED1 + w * 32 + lane * 8]); b += *reinterpret_cast<const f32x4*>(&lds[L_RED1 + w * 32 + lane * 8 + 4]); }
      const u32x4 qv = {pack2(a[0], a[1]), pack2(a[2], a[3]), pack2(b[0], b[1]), pack2(b[2], b[3])};
      __builtin_amdgcn_raw_buffer_store_b128(qv, rsW, (int)(WS_Q + (mod1 * 4096 + col0 + lane * 8) * 2), 0, AUX_SC1);
    }
    drain_vm();
    if (lane == 0) flag_store(ws, WS_FQ + j * 64, tag);
    wait_flags(ws, WS_FQ + (mod * 128 + h * 16 + (lane & 15)) * 64, lane < 16, tag);
    acquire_agent();
    const u32x4 qv = *reinterpret_cast<const u32x4*>(ws + WS_Q + (size_t)(mod * 4096 + h * 512 + lane * 8) * 2);
    *reinterpret_cast<u32x4*>(&lds[L_Q + lane * 4]) = qv;
  } else if (wave == 2 && lane == 0) {
    float g = 0.f;
#pragma unroll
    for (int w = 0; w < NWAVE; ++w) g += lds[L_GP + w];
    g = rbf(g);
    lds[L_GP + 8] = rbf(1.f / (1.f + __expf(-g)));
  }
  lds_barrier();

#pragma unroll
  for (int i = EARLY; i < NSLOT; ++i) Kr[i] = __builtin_amdgcn_raw_buffer_load_b128(rsK, voff, KV_SOFF(i), AUX_NT);
#pragma unroll
  for (int i = 0; i < NSLOT; ++i) Vr[i] = __builtin_amdgcn_raw_buffer_load_b128(rsV, voff, KV_SOFF(i), AUX_NT);
  const int hq3 = (j & 127) >> 3, row3 = (j & 127) * 32;
  const int rg3 = t / 56, cg3 = t - rg3 * 56;
  u32x4 Wo[4];
  {
    const u16* wo = (mod1 ? P.woB : P.woA) + (size_t)row3 * DM;
    const int tr = min(t, 447), rgc = tr / 56, cgc = tr - rgc * 56;
#pragma unroll
    for (int i = 0; i < 4; ++i) Wo[i] = __builtin_nontemporal_load(reinterpret_cast<const u32x4*>(wo + (size_t)(rgc + 8 * i) * DM + cgc * 8));
  }
  __builtin_amdgcn_sched_barrier(0);

  const int c = lane & 31, half = lane >> 5;
  u32x4 qk[2];
  qk[0] = *reinterpret_cast<const u32x4*>(&lds[L_Q + c * 4]);
  qk[1] = *reinterpret_cast<const u32x4*>(&lds[L_Q + 128 + c * 4]);
  const bool b3 = lane & 8, b2 = lane & 4, b1 = lane & 2;
  const int ulane = ((lane >> 4) & 1) * 4 + ((lane >> 3) & 1) * 2 + ((lane >> 2) & 1);
  constexpr float C2 = 0.0625f * 1.4426950408889634f;
  float tsc[3];
#pragma unroll
  for (int gi = 0; gi < 3; ++gi) {
    float xx[16];
#pragma unroll
    for (int u = 0; u < 8; ++u) {
      const int i = gi * 8 + u;
#pragma unroll
      for (int g = 0; g < 2; ++g) {
        float d = 0.f;
        if (i < NSLOT) {
#pragma unroll
          for (int k = 0; k < 4; ++k) d = dot2(Kr[i][k], qk[g][k], d);
        }
        xx[u * 2 + g] = d;
      }
    }
    float y[8], z[4], w2[2];
#pragma unroll
    for (int v = 0; v < 8; ++v) y[v] = swap16_add(xx[v], xx[v + 8]);
#pragma unroll
    for (int v = 0; v < 4; ++v) { const float keep = b3 ? y[v + 4] : y[v], send = b3 ? y[v] : y[v + 4]; z[v] = keep + dpp<XOR8>(send); }
#pragma unroll
    for (int v = 0; v < 2; ++v) { const float keep = b2 ? z[v + 2] : z[v], send = b2 ? z[v] : z[v + 2]; w2[v] = keep + dpp<XOR7>(send); }
    float sfin = (b1 ? w2[1] : w2[0]) + dpp<XOR2>(b1 ? w2[0] : w2[1]);
    sfin += dpp<XOR1>(sfin);
    const bool valid = (wave + 8 * (gi * 8 + ulane)) < np;
    tsc[gi] = valid ? rbf(sfin) * C2 : -INFINITY;
  }
  float mx = fmaxf(fmaxf(tsc[0], tsc[1]), tsc[2]);
  mx = fmaxf(mx, dpp<ROR4>(mx)); mx = fmaxf(mx, dpp<XOR8>(mx)); mx = x16_max(mx); mx = x32_max(mx);
  float pv[3], ls = 0.f;
#pragma unroll
  for (int gi = 0; gi < 3; ++gi) { pv[gi] = __builtin_amdgcn_exp2f(tsc[gi] - mx); ls += pv[gi]; }
  ls += dpp<ROR4>(ls); ls += dpp<XOR8>(ls); ls = swap16_add(ls, ls); ls = swap32_add(ls, ls);
  {
    float* pb = &lds[L_P + (wave * 2 + half) * 48];
    if (!(lane & 1)) {
#pragma unroll
      for (int gi = 0; gi < 3; ++gi) pb[(gi * 8 + ulane) * 2 + (b1 ? 1 : 0)] = pv[gi];
    }
    if (lane == 0 || lane == 2) { lds[L_ML + wave * 4 + (b1 ? 2 : 0)] = mx; lds[L_ML + wave * 4 + (b1 ? 2 : 0) + 1] = ls; }
  }
  lds_wait();
  float acc[2][8];
#pragma unroll
  for (int g = 0; g < 2; ++g)
#pragma unroll
    for (int k = 0; k < 8; ++k) acc[g][k] = 0.f;
  {
    const float* pb = &lds[L_P + (wave * 2 + half) * 48];
#pragma unroll
    for (int i2 = 0; i2 < (NSLOT + 1) / 2; ++i2) {
      const f32x4 pp = *reinterpret_cast<const f32x4*>(pb + i2 * 4);
#pragma unroll
      for (int e = 0; e < 2; ++e) {
        const int i = 2 * i2 + e;
        if (i < NSLOT) {
          float vf[8];
#pragma unroll
          for (int k = 0; k < 4; ++k) { vf[2 * k] = bflo(Vr[i][k]); vf[2 * k + 1] = bfhi(Vr[i][k]); }
#pragma unroll
          for (int g = 0; g < 2; ++g)
#pragma unroll
            for (int k = 0; k < 8; ++k) acc[g][k] = fmaf(pp[e * 2 + g], vf[k], acc[g][k]);
        }
      }
    }
  }
  {
    float o8[8];
#pragma unroll
    for (int k = 0; k < 8; ++k) o8[k] = swap32_add(acc[0][k], acc[1][k]);
    float* mg = &lds[L_MRG + wave * 512 + half * 256 + c * 8];
    *reinterpret_cast<f32x4*>(mg) = f32x4{o8[0], o8[1], o8[2], o8[3]};
    *reinterpret_cast<f32x4*>(mg + 4) = f32x4{o8[4], o8[5], o8[6], o8[7]};
  }
  lds_barrier();
  if (t < 128) {
    const int g = t >> 6, e = t & 63;
    float M = -INFINITY;
#pragma unroll
    for (int w = 0; w < NWAVE; ++w) M = fmaxf(M, lds[L_ML + w * 4 + g * 2]);
    float L = 0.f; f32x4 o = {0.f, 0.f, 0.f, 0.f};
#pragma unroll
    for (int w = 0; w < NWAVE; ++w) {
      const float wt = __builtin_amdgcn_exp2f(lds[L_ML + w * 4 + g * 2] - M);
      L = fmaf(wt, lds[L_ML + w * 4 + g * 2 + 1], L);
      o += wt * *reinterpret_cast<const f32x4*>(&lds[L_MRG + w * 512 + g * 256 + e * 4]);
    }
    __builtin_amdgcn_raw_buffer_store_b128(__builtin_bit_cast(u32x4, o), rsW, (int)(WS_O + j * 4096 + (g * 256 + e * 4) * 4), 0, AUX_SC1);
    if (e == 0) { const f32x4 ml = {M, L, 0.f, 0.f}; __builtin_amdgcn_raw_buffer_store_b128(__builtin_bit_cast(u32x4, ml), rsW, (int)(WS_O + j * 4096 + 2048 + g * 16), 0, AUX_SC1); }
  }
  drain_vm();
  lds_barrier();
  if (t == 0) flag_store(ws, WS_F1 + j * 64, tag);

  const int h3 = hq3 >> 1, g3 = hq3 & 1, ns3 = mod1 ? NSB : NSA;
  const int p0 = mod1 ? 8 * NSA + h3 * NSB : h3 * NSA;
  if (wave == 0) {
    wait_flags(ws, WS_F1 + (p0 + min(lane, ns3 - 1)) * 64, lane < ns3, tag);
    acquire_agent();
  }
  lds_barrier();
  {
    f32x4 ml = {-INFINITY, 0.f, 0.f, 0.f};
    if (lane < ns3) ml = *reinterpret_cast<const f32x4*>(ws + WS_O + (size_t)(p0 + lane) * 4096 + 2048 + g3 * 16);
    float M = ml[0];
    M = fmaxf(M, dpp<XOR1>(M)); M = fmaxf(M, dpp<XOR2>(M)); M = fmaxf(M, dpp<ROR4>(M)); M = fmaxf(M, dpp<XOR8>(M)); M = x16_max(M); M = x32_max(M);
    const float wt = (lane < ns3) ? __builtin_amdgcn_exp2f(ml[0] - M) : 0.f;
    float L = wt * ml[1];
    L += dpp<XOR1>(L); L += dpp<XOR2>(L); L += dpp<ROR4>(L); L += dpp<XOR8>(L); L = swap16_add(L, L); L = swap32_add(L, L);
    const float cw = wt / L;
    const int sidx = t >> 3;
    const float cs = __shfl(cw, sidx & 63, 64);
    f32x4 v = {0.f, 0.f, 0.f, 0.f};
    if (sidx < ns3) v = cs * *reinterpret_cast<const f32x4*>(ws + WS_O + (size_t)(p0 + sidx) * 4096 + (g3 * 256 + (j & 7) * 32 + (t & 7) * 4) * 4);
#pragma unroll
    for (int k = 0; k < 4; ++k) { float q = v[k]; q += dpp<XOR8>(q); q = swap16_add(q, q); q = swap32_add(q, q); v[k] = q; }
    if (lane < 8) *reinterpret_cast<f32x4*>(&lds[L_RED2 + wave * 32 + lane * 4]) = v;
  }
  lds_barrier();
  if (t < 448) {
    float pacc[8];
#pragma unroll
    for (int k = 0; k < 8; ++k) pacc[k] = 0.f;
#pragma unroll
    for (int i = 0; i < 4; ++i) {
      const int row = rg3 + 8 * i;
      float ov = 0.f;
#pragma unroll
      for (int w = 0; w < 4; ++w) ov += lds[L_RED2 + w * 32 + row];
      ov = rbf(ov);
#pragma unroll
      for (int k = 0; k < 4; ++k) { pacc[2 * k] = fmaf(ov, bflo(Wo[i][k]), pacc[2 * k]); pacc[2 * k + 1] = fmaf(ov, bfhi(Wo[i][k]), pacc[2 * k + 1]); }
    }
    float* r3 = &lds[L_RED3 + rg3 * 448 + cg3 * 8];
    *reinterpret_cast<f32x4*>(r3) = f32x4{pacc[0], pacc[1], pacc[2], pacc[3]};
    *reinterpret_cast<f32x4*>(r3 + 4) = f32x4{pacc[4], pacc[5], pacc[6], pacc[7]};
  }
  lds_barrier();
  if (t < 112) {
    f32x4 sacc = *reinterpret_cast<const f32x4*>(&lds[L_RED3 + t * 4]);
#pragma unroll
    for (int rg = 1; rg < 8; ++rg) sacc += *reinterpret_cast<const f32x4*>(&lds[L_RED3 + rg * 448 + t * 4]);
    __builtin_amdgcn_raw_buffer_store_b128(__builtin_bit_cast(u32x4, sacc), rsW, (int)(WS_R + j * 2048 + t * 16), 0, AUX_SC1);
  }
  drain_vm();
  lds_barrier();
  if (t == 0) flag_store(ws, WS_F2 + j * 64, tag);

  if (j < 112) {
    if (wave == 0) {
      wait_flags(ws, WS_F2 + lane * 64, true, tag);
      wait_flags(ws, WS_F2 + (lane + 64) * 64, true, tag);
      wait_flags(ws, WS_F2 + (lane + 128) * 64, true, tag);
      wait_flags(ws, WS_F2 + (lane + 192) * 64, true, tag);
      acquire_agent();
    }
    lds_barrier();
    if (t < 256) {
      f32x4 v = *reinterpret_cast<const f32x4*>(ws + WS_R + (size_t)t * 2048 + j * 16);
#pragma unroll
      for (int k = 0; k < 4; ++k) {
        float q = v[k];
        q += dpp<XOR1>(q); q += dpp<XOR2>(q); q += dpp<ROR4>(q); q += dpp<XOR8>(q); q = swap16_add(q, q); q = swap32_add(q, q);
        v[k] = q;
      }
      if (lane == 0) *reinterpret_cast<f32x4*>(&lds[L_FIN + wave * 4]) = v;
    }
    lds_barrier();
    if (t == 0) {
      const float g = lds[L_GP + 8], og = rbf(1.f - g);
      u32 o[4];
#pragma unroll
      for (int k = 0; k < 4; ++k) {
        const float a = rbf(lds[L_FIN + k] + lds[L_FIN + 4 + k]), b = rbf(lds[L_FIN + 8 + k] + lds[L_FIN + 12 + k]);
        o[k] = f2bf_bits(rbf(g * a) + rbf(og * b));
      }
      *reinterpret_cast<u32x2*>(P.out + j * 4) = u32x2{o[0] | (o[1] << 16), o[2] | (o[3] << 16)};
      if (j == 0) *reinterpret_cast<u64*>(ws + WS_EPOCH) = tag;
    }
  }
}
}

extern "C" void kernel_launch(void* const* d_in, const int* in_sizes, int n_in, void* d_out, int out_size, void* d_ws, size_t ws_size,
                              hipStream_t stream) {
  Ptrs P;
  P.x = (const u16*)d_in[0]; P.kA = (const u16*)d_in[1]; P.vA = (const u16*)d_in[2]; P.wqA = (const u16*)d_in[3]; P.woA = (const u16*)d_in[4];
  P.kB = (const u16*)d_in[6]; P.vB = (const u16*)d_in[7]; P.wqB = (const u16*)d_in[8]; P.woB = (const u16*)d_in[9]; P.gw = (const u16*)d_in[10];
  P.out = (u16*)d_out; P.ws = (unsigned char*)d_ws;
  hipLaunchKernelGGL(fused_decode, dim3(256), dim3(NTHR), 0, stream, P);
}
```

```cpp
#include <hip/hip_runtime.h>
#include <hip/hip_bf16.h>
#include <stdint.h>

namespace {
typedef unsigned short u16;
typedef unsigned int u32;
typedef unsigned long long u64;
typedef __attribute__((ext_vector_type(4))) unsigned u32x4;
typedef __attribute__((ext_vector_type(2))) unsigned u32x2;
typedef __attribute__((ext_vector_type(4))) float f32x4;
typedef __attribute__((ext_vector_type(2))) float f32x2;
typedef __attribute__((ext_vector_type(2))) __bf16 bf16x2;

constexpr int DM = 448, SK = 8192, DH = 256, WINDOW = 2048;
constexpr int NSA = 7, NSB = 25;
constexpr int PERA = 294, PERB = 328;
constexpr int NTHR = 512, NWAVE = 8;
constexpr int NSLOT = 21;
#define EARLY_K 6
constexpr int EARLY = EARLY_K;

constexpr u32 WS_EPOCH = 0;
constexpr u32 WS_FQ = 0x1000;
constexpr u32 WS_F1 = 0x8000;
constexpr u32 WS_F2 = 0xC000;
constexpr u32 WS_Q = 0x10000;
constexpr u32 WS_O = 0x20000;
constexpr u32 WS_R = 0x200000;
constexpr u32 WS_BYTES = 0x300000;
constexpr int AUX_SC1 = 16;
constexpr int AUX_NT = 2;
constexpr unsigned SPIN_CAP = 1u << 17;

constexpr int L_RED1 = 0;
constexpr int L_GP = 256;
constexpr int L_Q = 288;
constexpr int L_P = 544;
constexpr int L_ML = 1312;
constexpr int L_MRG = 1344;
constexpr int L_RED2 = L_MRG;
constexpr int L_RED3 = L_MRG + 256;
constexpr int L_FIN = L_MRG + 3840;
constexpr int L_TOTAL = L_MRG + 4096;

__device__ __forceinline__ float bf2f(u16 h) { return __uint_as_float(((u32)h) << 16); }
__device__ __forceinline__ float bflo(u32 w) { return __uint_as_float(w << 16); }
__device__ __forceinline__ float bfhi(u32 w) { return __uint_as_float(w & 0xffff0000u); }
__device__ __forceinline__ u32 f2bf_bits(float f) {
  return (u32)__builtin_bit_cast(u16, (__bf16)f);
}
__device__ __forceinline__ float rbf(float f) { return __uint_as_float(f2bf_bits(f) << 16); }
__device__ __forceinline__ u32 pack2(float lo, float hi) { return f2bf_bits(lo) | (f2bf_bits(hi) << 16); }
__device__ __forceinline__ float dot2(u32 a, u32 b, float c) {
  return __builtin_amdgcn_fdot2_f32_bf16(__builtin_bit_cast(bf16x2, a), __builtin_bit_cast(bf16x2, b), c, false);
}
template <int CTRL> __device__ __forceinline__ float dpp(float x) {
  return __builtin_bit_cast(float, __builtin_amdgcn_mov_dpp(__builtin_bit_cast(int, x), CTRL, 0xf, 0xf, true));
}
constexpr int XOR1 = 0xB1, XOR2 = 0x4E, XOR7 = 0x141, XOR8 = 0x128, ROR4 = 0x124;
__device__ __forceinline__ float swap16_add(float a, float b) {
  auto s = __builtin_amdgcn_permlane16_swap(__float_as_uint(a), __float_as_uint(b), false, false);
  return __uint_as_float(s[0]) + __uint_as_float(s[1]);
}
__device__ __forceinline__ float swap32_add(float a, float b) {
  auto s = __builtin_amdgcn_permlane32_swap(__float_as_uint(a), __float_as_uint(b), false, false);
  return __uint_as_float(s[0]) + __uint_as_float(s[1]);
}
__device__ __forceinline__ float x16_max(float x) {
  auto s = __builtin_amdgcn_permlane16_swap(__float_as_uint(x), __float_as_uint(x), false, false);
  return fmaxf(__uint_as_float(s[0]), __uint_as_float(s[1]));
}
__device__ __forceinline__ float x32_max(float x) {
  auto s = __builtin_amdgcn_permlane32_swap(__float_as_uint(x), __float_as_uint(x), false, false);
  return fmaxf(__uint_as_float(s[0]), __uint_as_float(s[1]));
}
__device__ __forceinline__ void lds_barrier() { asm volatile("s_waitcnt lgkmcnt(0)\n\ts_barrier" ::: "memory"); }
__device__ __forceinline__ void lds_wait() { asm volatile("s_waitcnt lgkmcnt(0)" ::: "memory"); }
__device__ __forceinline__ void drain_vm() { asm volatile("s_waitcnt vmcnt(0)" ::: "memory"); }
__device__ __forceinline__ void flag_store(unsigned char* ws, u32 off, u64 tag) {
  __hip_atomic_store(reinterpret_cast<u64*>(ws + off), tag, __ATOMIC_RELAXED, __HIP_MEMORY_SCOPE_AGENT);
}
__device__ __forceinline__ u64 flag_load(unsigned char* ws, u32 off) {
  return __hip_atomic_load(reinterpret_cast<u64*>(ws + off), __ATOMIC_RELAXED, __HIP_MEMORY_SCOPE_AGENT);
}
__device__ __forceinline__ void wait_flags(unsigned char* ws, u32 off, bool mine, u64 tag) {
  for (unsigned spins = 0; spins < SPIN_CAP; ++spins) {
    const bool ok = !mine || flag_load(ws, off) == tag;
    if (__all(ok)) break;
    __builtin_amdgcn_s_sleep(2);
  }
}
__device__ __forceinline__ void acquire_agent() {
  __builtin_amdgcn_fence(__ATOMIC_ACQUIRE, "agent");
  asm volatile("s_waitcnt vmcnt(0)" ::: "memory");
}

#define STAMP(k) do {} while (0)

struct Ptrs {
  const u16* x; const u16* kA; const u16* vA; const u16* wqA; const u16* woA;
  const u16* kB; const u16* vB; const u16* wqB; const u16* woB; const u16* gw;
  u16* out; unsigned char* ws;
};

__global__ void __launch_bounds__(NTHR, 2) fused_decode(Ptrs P) {
  __shared__ __attribute__((aligned(16))) float lds[L_TOTAL];
  const int j = blockIdx.x, t = threadIdx.x, lane = t & 63;
  const int wave = __builtin_amdgcn_readfirstlane(t >> 6);
  unsigned char* ws = P.ws;
  STAMP(0);
  const u64 tag = *reinterpret_cast<const u64*>(ws + WS_EPOCH) + 1ull;
  const __amdgpu_buffer_rsrc_t rsW = __builtin_amdgcn_make_buffer_rsrc(ws, 0, (int)WS_BYTES, 0x00020000);

  int mod, h, s, k0, k1;
  if (j < 8 * NSA) { mod = 0; h = j / NSA; s = j - h * NSA; k0 = s * PERA; k1 = min(k0 + PERA, WINDOW); k0 += SK - WINDOW; k1 += SK - WINDOW; }
  else { const int jj = j - 8 * NSA; mod = 1; h = jj / NSB; s = jj - h * NSB; k0 = s * PERB; k1 = min(k0 + PERB, SK); }
  const int np = (k1 - k0) >> 1;
  const u16* kbase = (mod ? P.kB : P.kA) + (size_t)h * SK * DH;
  const u16* vbase = (mod ? P.vB : P.vA) + (size_t)h * SK * DH;
  const __amdgpu_buffer_rsrc_t rsK = __builtin_amdgcn_make_buffer_rsrc((void*)kbase, 0, SK * DH * 2, 0x00020000);
  const __amdgpu_buffer_rsrc_t rsV = __builtin_amdgcn_make_buffer_rsrc((void*)vbase, 0, SK * DH * 2, 0x00020000);
  const int voff = (lane >> 5) * 512 + (lane & 31) * 16;
#define KV_SOFF(i) ((k0 + 2 * min(wave + 8 * (i), np - 1)) * 512)

  const int mod1 = j >> 7, col0 = (j & 127) * 32;
  const u16* wq = mod1 ? P.wqB : P.wqA;
  const int seg = t & 3, r0 = t >> 2;
  u32x4 wv[4]; u16 xr[4];
#pragma unroll
  for (int i = 0; i < 4; ++i) {
    const int row = min(r0 + 128 * i, DM - 1);
    wv[i] = __builtin_nontemporal_load(reinterpret_cast<const u32x4*>(wq + (size_t)row * 4096 + col0 + seg * 8));
    xr[i] = P.x[row];
  }
  const int tg = min(t, DM - 1);
  const u16 gxr = P.x[tg], gwr = P.gw[tg];

  u32x4 Kr[NSLOT], Vr[NSLOT];
#pragma unroll
  for (int i = 0; i < EARLY; ++i) Kr[i] = __builtin_amdgcn_raw_buffer_load_b128(rsK, voff, KV_SOFF(i), AUX_NT);
  __builtin_amdgcn_sched_barrier(0);

  STAMP(15);
  const float gp = (t < DM) ? rbf(bf2f(gxr) * bf2f(gwr)) : 0.f;
  float xv[4];
#pragma unroll
  for (int i = 0; i < 4; ++i) xv[i] = (r0 + 128 * i < DM) ? bf2f(xr[i]) : 0.f;
  {
    float acc[8];
#pragma unroll
    for (int k = 0; k < 8; ++k) acc[k] = 0.f;
#pragma unroll
    for (int i = 0; i < 4; ++i)
#pragma unroll
      for (int k = 0; k < 4; ++k) { acc[2 * k] = fmaf(xv[i], bflo(wv[i][k]), acc[2 * k]); acc[2 * k + 1] = fmaf(xv[i], bfhi(wv[i][k]), acc[2 * k + 1]); }
#pragma unroll
    for (int k = 0; k < 8; ++k) {
      float v = acc[k];
      v += dpp<ROR4>(v); v += dpp<XOR8>(v);
      v = swap16_add(v, v); v = swap32_add(v, v);
      acc[k] = v;
    }
    if (lane < 4) {
      *reinterpret_cast<f32x4*>(&lds[L_RED1 + wave * 32 + lane * 8]) = f32x4{acc[0], acc[1], acc[2], acc[3]};
      *reinterpret_cast<f32x4*>(&lds[L_RED1 + wave * 32 + lane * 8 + 4]) = f32x4{acc[4], acc[5], acc[6], acc[7]};
    }
    float g = gp;
    g += dpp<XOR1>(g); g += dpp<XOR2>(g); g += dpp<ROR4>(g); g += dpp<XOR8>(g);
    g = swap16_add(g, g); g = swap32_add(g, g);
    if (lane == 0) lds[L_GP + wave] = g;
  }
  lds_barrier();
  STAMP(1);
  if (wave == 0) {
    if (lane < 4) {
      f32x4 a = *reinterpret_cast<const f32x4*>(&lds[L_RED1 + lane * 8]);
      f32x4 b = *reinterpret_cast<const f32x4*>(&lds[L_RED1 + lane * 8 + 4]);
#pragma unroll
      for (int w = 1; w < NWAVE; ++w) { a += *reinterpret_cast<const f32x4*>(&lds[L_RED1 + w * 32 + lane * 8]); b += *reinterpret_cast<const f32x4*>(&lds[L_RED1 + w * 32 + lane * 8 + 4]); }
      const u32x4 qv = {pack2(a[0], a[1]), pack2(a[2], a[3]), pack2(b[0], b[1]), pack2(b[2], b[3])};
      __builtin_amdgcn_raw_buffer_store_b128(qv, rsW, (int)(WS_Q + (mod1 * 4096 + col0 + lane * 8) * 2), 0, AUX_SC1);
    }
    drain_vm();
    if (lane == 0) flag_store(ws, WS_FQ + j * 64, tag);
    STAMP(2);
    wait_flags(ws, WS_FQ + (mod * 128 + h * 16 + (lane & 15)) * 64, lane < 16, tag);
    acquire_agent();
    const u32x4 qv = *reinterpret_cast<const u32x4*>(ws + WS_Q + (size_t)(mod * 4096 + h * 512 + lane * 8) * 2);
    *reinterpret_cast<u32x4*>(&lds[L_Q + lane * 4]) = qv;
    STAMP(3);
  } else if (wave == 2 && lane == 0) {
    float g = 0.f;
#pragma unroll
    for (int w = 0; w < NWAVE; ++w) g += lds[L_GP + w];
    g = rbf(g);
    lds[L_GP + 8] = rbf(1.f / (1.f + __expf(-g)));
  }
  lds_barrier();

#pragma unroll
  for (int i = EARLY; i < NSLOT; ++i) Kr[i] = __builtin_amdgcn_raw_buffer_load_b128(rsK, voff, KV_SOFF(i), AUX_NT);
#pragma unroll
  for (int i = 0; i < NSLOT; ++i) Vr[i] = __builtin_amdgcn_raw_buffer_load_b128(rsV, voff, KV_SOFF(i), AUX_NT);
  const int hq3 = (j & 127) >> 3, row3 = (j & 127) * 32;
  const int rg3 = t / 56, cg3 = t - rg3 * 56;
  u32x4 Wo[4];
  {
    const u16* wo = (mod1 ? P.woB : P.woA) + (size_t)row3 * DM;
    const int tr = min(t, 447), rgc = tr / 56, cgc = tr - rgc * 56;
#pragma unroll
    for (int i = 0; i < 4; ++i) Wo[i] = __builtin_nontemporal_load(reinterpret_cast<const u32x4*>(wo + (size_t)(rgc + 8 * i) * DM + cgc * 8));
  }
  __builtin_amdgcn_sched_barrier(0);
  STAMP(4);

  const int c = lane & 31, half = lane >> 5;
  u32x4 qk[2];
  qk[0] = *reinterpret_cast<const u32x4*>(&lds[L_Q + c * 4]);
  qk[1] = *reinterpret_cast<const u32x4*>(&lds[L_Q + 128 + c * 4]);
  const bool b3 = lane & 8, b2 = lane & 4, b1 = lane & 2;
  const int ulane = ((lane >> 4) & 1) * 4 + ((lane >> 3) & 1) * 2 + ((lane >> 2) & 1);
  constexpr float C2 = 0.0625f * 1.4426950408889634f;
  float tsc[3];
#pragma unroll
  for (int gi = 0; gi < 3; ++gi) {
    float xx[16];
#pragma unroll
    for (int u = 0; u < 8; ++u) {
      const int i = gi * 8 + u;
#pragma unroll
      for (int g = 0; g < 2; ++g) {
        float d = 0.f;
        if (i < NSLOT) {
#pragma unroll
          for (int k = 0; k < 4; ++k) d = dot2(Kr[i][k], qk[g][k], d);
        }
        xx[u * 2 + g] = d;
      }
    }
    float y[8], z[4], w2[2];
#pragma unroll
    for (int v = 0; v < 8; ++v) y[v] = swap16_add(xx[v], xx[v + 8]);
#pragma unroll
    for (int v = 0; v < 4; ++v) { const float keep = b3 ? y[v + 4] : y[v], send = b3 ? y[v] : y[v + 4]; z[v] = keep + dpp<XOR8>(send); }
#pragma unroll
    for (int v = 0; v < 2; ++v) { const float keep = b2 ? z[v + 2] : z[v], send = b2 ? z[v] : z[v + 2]; w2[v] = keep + dpp<XOR7>(send); }
    float sfin = (b1 ? w2[1] : w2[0]) + dpp<XOR2>(b1 ? w2[0] : w2[1]);
    sfin += dpp<XOR1>(sfin);
    const bool valid = (wave + 8 * (gi * 8 + ulane)) < np;
    tsc[gi] = valid ? rbf(sfin) * C2 : -INFINITY;
  }
  float mx = fmaxf(fmaxf(tsc[0], tsc[1]), tsc[2]);
  mx = fmaxf(mx, dpp<ROR4>(mx)); mx = fmaxf(mx, dpp<XOR8>(mx)); mx = x16_max(mx); mx = x32_max(mx);
  float pv[3], ls = 0.f;
#pragma unroll
  for (int gi = 0; gi < 3; ++gi) { pv[gi] = __builtin_amdgcn_exp2f(tsc[gi] - mx); ls += pv[gi]; }
  ls += dpp<ROR4>(ls); ls += dpp<XOR8>(ls); ls = swap16_add(ls, ls); ls = swap32_add(ls, ls);
  {
    float* pb = &lds[L_P + (wave * 2 + half) * 48];
    if (!(lane & 1)) {
#pragma unroll
      for (int gi = 0; gi < 3; ++gi) pb[(gi * 8 + ulane) * 2 + (b1 ? 1 : 0)] = pv[gi];
    }
    if (lane == 0 || lane == 2) { lds[L_ML + wave * 4 + (b1 ? 2 : 0)] = mx; lds[L_ML + wave * 4 + (b1 ? 2 : 0) + 1] = ls; }
  }
  lds_wait();
  STAMP(5);
  float acc[2][8];
#pragma unroll
  for (int g = 0; g < 2; ++g)
#pragma unroll
    for (int k = 0; k < 8; ++k) acc[g][k] = 0.f;
  {
    const float* pb = &lds[L_P + (wave * 2 + half) * 48];
#pragma unroll
    for (int i2 = 0; i2 < (NSLOT + 1) / 2; ++i2) {
      const f32x4 pp = *reinterpret_cast<const f32x4*>(pb + i2 * 4);
#pragma unroll
      for (int e = 0; e < 2; ++e) {
        const int i = 2 * i2 + e;
        if (i < NSLOT) {
          float vf[8];
#pragma unroll
          for (int k = 0; k < 4; ++k) { vf[2 * k] = bflo(Vr[i][k]); vf[2 * k + 1] = bfhi(Vr[i][k]); }
#pragma unroll
          for (int g = 0; g < 2; ++g)
#pragma unroll
            for (int k = 0; k < 8; ++k) acc[g][k] = fmaf(pp[e * 2 + g], vf[k], acc[g][k]);
        }
      }
    }
  }
  {
    float o8[8];
#pragma unroll
    for (int k = 0; k < 8; ++k) o8[k] = swap32_add(acc[0][k], acc[1][k]);
    float* mg = &lds[L_MRG + wave * 512 + half * 256 + c * 8];
    *reinterpret_cast<f32x4*>(mg) = f32x4{o8[0], o8[1], o8[2], o8[3]};
    *reinterpret_cast<f32x4*>(mg + 4) = f32x4{o8[4], o8[5], o8[6], o8[7]};
  }
  lds_barrier();
  STAMP(6);
  if (t < 128) {
    const int g = t >> 6, e = t & 63;
    float M = -INFINITY;
#pragma unroll
    for (int w = 0; w < NWAVE; ++w) M = fmaxf(M, lds[L_ML + w * 4 + g * 2]);
    float L = 0.f; f32x4 o = {0.f, 0.f, 0.f, 0.f};
#pragma unroll
    for (int w = 0; w < NWAVE; ++w) {
      const float wt = __builtin_amdgcn_exp2f(lds[L_ML + w * 4 + g * 2] - M);
      L = fmaf(wt, lds[L_ML + w * 4 + g * 2 + 1], L);
      o += wt * *reinterpret_cast<const f32x4*>(&lds[L_MRG + w * 512 + g * 256 + e * 4]);
    }
    __builtin_amdgcn_raw_buffer_store_b128(__builtin_bit_cast(u32x4, o), rsW, (int)(WS_O + j * 4096 + (g * 256 + e * 4) * 4), 0, AUX_SC1);
    if (e == 0) { const f32x4 ml = {M, L, 0.f, 0.f}; __builtin_amdgcn_raw_buffer_store_b128(__builtin_bit_cast(u32x4, ml), rsW, (int)(WS_O + j * 4096 + 2048 + g * 16), 0, AUX_SC1); }
  }
  drain_vm();
  lds_barrier();
  if (t == 0) flag_store(ws, WS_F1 + j * 64, tag);
  STAMP(7);

  const int h3 = hq3 >> 1, g3 = hq3 & 1, ns3 = mod1 ? NSB : NSA;
  const int p0 = mod1 ? 8 * NSA + h3 * NSB : h3 * NSA;
  if (wave == 0) {
    wait_flags(ws, WS_F1 + (p0 + min(lane, ns3 - 1)) * 64, lane < ns3, tag);
    acquire_agent();
  }
  lds_barrier();
  STAMP(8);
  {
    f32x4 ml = {-INFINITY, 0.f, 0.f, 0.f};
    if (lane < ns3) ml = *reinterpret_cast<const f32x4*>(ws + WS_O + (size_t)(p0 + lane) * 4096 + 2048 + g3 * 16);
    float M = ml[0];
    M = fmaxf(M, dpp<XOR1>(M)); M = fmaxf(M, dpp<XOR2>(M)); M = fmaxf(M, dpp<ROR4>(M)); M = fmaxf(M, dpp<XOR8>(M)); M = x16_max(M); M = x32_max(M);
    const float wt = (lane < ns3) ? __builtin_amdgcn_exp2f(ml[0] - M) : 0.f;
    float L = wt * ml[1];
    L += dpp<XOR1>(L); L += dpp<XOR2>(L); L += dpp<ROR4>(L); L += dpp<XOR8>(L); L = swap16_add(L, L); L = swap32_add(L, L);
    const float cw = wt / L;
    const int sidx = t >> 3;
    const float cs = __shfl(cw, sidx & 63, 64);
    f32x4 v = {0.f, 0.f, 0.f, 0.f};
    if (sidx < ns3) v = cs * *reinterpret_cast<const f32x4*>(ws + WS_O + (size_t)(p0 + sidx) * 4096 + (g3 * 256 + (j & 7) * 32 + (t & 7) * 4) * 4);
#pragma unroll
    for (int k = 0; k < 4; ++k) { float q = v[k]; q += dpp<XOR8>(q); q = swap16_add(q, q); q = swap32_add(q, q); v[k] = q; }
    if (lane < 8) *reinterpret_cast<f32x4*>(&lds[L_RED2 + wave * 32 + lane * 4]) = v;
  }
  lds_barrier();
  STAMP(9);
  if (t < 448) {
    float pacc[8];
#pragma unroll
    for (int k = 0; k < 8; ++k) pacc[k] = 0.f;
#pragma unroll
    for (int i = 0; i < 4; ++i) {
      const int row = rg3 + 8 * i;
      float ov = 0.f;
#pragma unroll
      for (int w = 0; w < 4; ++w) ov += lds[L_RED2 + w * 32 + row];
      ov = rbf(ov);
#pragma unroll
      for (int k = 0; k < 4; ++k) { pacc[2 * k] = fmaf(ov, bflo(Wo[i][k]), pacc[2 * k]); pacc[2 * k + 1] = fmaf(ov, bfhi(Wo[i][k]), pacc[2 * k + 1]); }
    }
    float* r3 = &lds[L_RED3 + rg3 * 448 + cg3 * 8];
    *reinterpret_cast<f32x4*>(r3) = f32x4{pacc[0], pacc[1], pacc[2], pacc[3]};
    *reinterpret_cast<f32x4*>(r3 + 4) = f32x4{pacc[4], pacc[5], pacc[6], pacc[7]};
  }
  lds_barrier();
  STAMP(10);
  if (t < 112) {
    f32x4 sacc = *reinterpret_cast<const f32x4*>(&lds[L_RED3 + t * 4]);
#pragma unroll
    for (int rg = 1; rg < 8; ++rg) sacc += *reinterpret_cast<const f32x4*>(&lds[L_RED3 + rg * 448 + t * 4]);
    __builtin_amdgcn_raw_buffer_store_b128(__builtin_bit_cast(u32x4, sacc), rsW, (int)(WS_R + j * 2048 + t * 16), 0, AUX_SC1);
  }
  drain_vm();
  lds_barrier();
  if (t == 0) flag_store(ws, WS_F2 + j * 64, tag);
  STAMP(11);

  if (j < 112) {
    if (wave == 0) {
      wait_flags(ws, WS_F2 + lane * 64, true, tag);
      wait_flags(ws, WS_F2 + (lane + 64) * 64, true, tag);
      wait_flags(ws, WS_F2 + (lane + 128) * 64, true, tag);
      wait_flags(ws, WS_F2 + (lane + 192) * 64, true, tag);
      STAMP(12);
      acquire_agent();
    }
    lds_barrier();
    if (t < 256) {
      f32x4 v = *reinterpret_cast<const f32x4*>(ws + WS_R + (size_t)t * 2048 + j * 16);
#pragma unroll
      for (int k = 0; k < 4; ++k) {
        float q = v[k];
        q += dpp<XOR1>(q); q += dpp<XOR2>(q); q += dpp<ROR4>(q); q += dpp<XOR8>(q); q = swap16_add(q, q); q = swap32_add(q, q);
        v[k] = q;
      }
      if (lane == 0) *reinterpret_cast<f32x4*>(&lds[L_FIN + wave * 4]) = v;
    }
    lds_barrier();
    if (t == 0) {
      const float g = lds[L_GP + 8], og = rbf(1.f - g);
      u32 o[4];
#pragma unroll
      for (int k = 0; k < 4; ++k) {
        const float a = rbf(lds[L_FIN + k] + lds[L_FIN + 4 + k]), b = rbf(lds[L_FIN + 8 + k] + lds[L_FIN + 12 + k]);
        o[k] = f2bf_bits(rbf(g * a) + rbf(og * b));
      }
      *reinterpret_cast<u32x2*>(P.out + j * 4) = u32x2{o[0] | (o[1] << 16), o[2] | (o[3] << 16)};
      if (j == 0) *reinterpret_cast<u64*>(ws + WS_EPOCH) = tag;
    }
  }
  STAMP(13);
}
}

extern "C" void kernel_launch(void* const* d_in, const int* in_sizes, int n_in, void* d_out, int out_size, void* d_ws, size_t ws_size,
                              hipStream_t stream) {
  Ptrs P;
  P.x = (const u16*)d_in[0]; P.kA = (const u16*)d_in[1]; P.vA = (const u16*)d_in[2]; P.wqA = (const u16*)d_in[3]; P.woA = (const u16*)d_in[4];
  P.kB = (const u16*)d_in[6]; P.vB = (const u16*)d_in[7]; P.wqB = (const u16*)d_in[8]; P.woB = (const u16*)d_in[9]; P.gw = (const u16*)d_in[10];
  P.out = (u16*)d_out; P.ws = (unsigned char*)d_ws;
  hipLaunchKernelGGL(fused_decode, dim3(256), dim3(NTHR), 0, stream, P);
}
```

```cpp
#include <hip/hip_runtime.h>
#include <hip/hip_bf16.h>
#include <stdint.h>

namespace {
typedef unsigned short u16;
typedef unsigned int u32;
typedef unsigned long long u64;
typedef __attribute__((ext_vector_type(4))) unsigned u32x4;
typedef __attribute__((ext_vector_type(2))) unsigned u32x2;
typedef __attribute__((ext_vector_type(4))) float f32x4;
typedef __attribute__((ext_vector_type(2))) __bf16 bf16x2;

constexpr int DM = 448, SK = 8192, DH = 256, WINDOW = 2048;
constexpr int NSA = 7, NSB = 25;
constexpr int PERA = 294, PERB = 328;
constexpr int NTHR = 512, NWAVE = 8;
constexpr int NSLOT = 21;
#define EARLY_K 0
constexpr int EARLY = EARLY_K;

constexpr u32 WS_EPOCH = 0;
constexpr u32 WS_RDY = 0x1000;
constexpr u32 WS_QW = 0x10000;
constexpr u32 WS_I1 = 0x100000;
constexpr u32 WS_I2 = 0x300000;
constexpr u32 WS_BYTES = 0x400000;
constexpr int AUX_SC1 = 16;
#define KV_AUX 2
constexpr int AUX_NT = KV_AUX;
constexpr unsigned SPIN_CAP = 1u << 17;
constexpr u64 FULL = 1ull << 32;

constexpr int L_RED1 = 0;
constexpr int L_GP = 256;
constexpr int L_Q = 288;
constexpr int L_P = 544;
constexpr int L_ML = 1312;
constexpr int L_MRG = 1344;
constexpr int L_ST1 = L_MRG + 4096;
constexpr int L_RED2 = L_ST1 + 1024;
constexpr int L_RED3 = L_RED2 + 256;
constexpr int L_FIN = L_RED3 + 3584;
constexpr int L_TOTAL = L_FIN + 16;

__device__ __forceinline__ float bf2f(u16 h) { return __uint_as_float(((u32)h) << 16); }
__device__ __forceinline__ float bflo(u32 w) { return __uint_as_float(w << 16); }
__device__ __forceinline__ float bfhi(u32 w) { return __uint_as_float(w & 0xffff0000u); }
__device__ __forceinline__ u32 f2bf_bits(float f) { return (u32)__builtin_bit_cast(u16, (__bf16)f); }
__device__ __forceinline__ float rbf(float f) { return __uint_as_float(f2bf_bits(f) << 16); }
__device__ __forceinline__ u32 pack2(float lo, float hi) { return f2bf_bits(lo) | (f2bf_bits(hi) << 16); }
__device__ __forceinline__ float dot2(u32 a, u32 b, float c) {
  return __builtin_amdgcn_fdot2_f32_bf16(__builtin_bit_cast(bf16x2, a), __builtin_bit_cast(bf16x2, b), c, false);
}
template <int CTRL> __device__ __forceinline__ float dpp(float x) {
  return __builtin_bit_cast(float, __builtin_amdgcn_mov_dpp(__builtin_bit_cast(int, x), CTRL, 0xf, 0xf, true));
}
constexpr int XOR1 = 0xB1, XOR2 = 0x4E, XOR7 = 0x141, XOR8 = 0x128, ROR4 = 0x124;
__device__ __forceinline__ float swap16_add(float a, float b) {
  auto s = __builtin_amdgcn_permlane16_swap(__float_as_uint(a), __float_as_uint(b), false, false);
  return __uint_as_float(s[0]) + __uint_as_float(s[1]);
}
__device__ __forceinline__ float swap32_add(float a, float b) {
  auto s = __builtin_amdgcn_permlane32_swap(__float_as_uint(a), __float_as_uint(b), false, false);
  return __uint_as_float(s[0]) + __uint_as_float(s[1]);
}
__device__ __forceinline__ float x16_max(float x) {
  auto s = __builtin_amdgcn_permlane16_swap(__float_as_uint(x), __float_as_uint(x), false, false);
  return fmaxf(__uint_as_float(s[0]), __uint_as_float(s[1]));
}
__device__ __forceinline__ float x32_max(float x) {
  auto s = __builtin_amdgcn_permlane32_swap(__float_as_uint(x), __float_as_uint(x), false, false);
  return fmaxf(__uint_as_float(s[0]), __uint_as_float(s[1]));
}
__device__ __forceinline__ void lds_barrier() { asm volatile("s_waitcnt lgkmcnt(0)\n\ts_barrier" ::: "memory"); }
__device__ __forceinline__ void lds_wait() { asm volatile("s_waitcnt lgkmcnt(0)" ::: "memory"); }
__device__ __forceinline__ void drain_vm() { asm volatile("s_waitcnt vmcnt(0)" ::: "memory"); }
__device__ __forceinline__ void word_store(unsigned char* ws, u32 off, u64 v) {
  __hip_atomic_store(reinterpret_cast<u64*>(ws + off), v, __ATOMIC_RELAXED, __HIP_MEMORY_SCOPE_AGENT);
}
__device__ __forceinline__ u64 word_load(unsigned char* ws, u32 off) {
  return __hip_atomic_load(reinterpret_cast<u64*>(ws + off), __ATOMIC_RELAXED, __HIP_MEMORY_SCOPE_AGENT);
}

#define STAMP(k) do {} while (0)

struct Ptrs {
  const u16* x; const u16* kA; const u16* vA; const u16* wqA; const u16* woA;
  const u16* kB; const u16* vB; const u16* wqB; const u16* woB; const u16* gw;
  u16* out; unsigned char* ws;
};

__global__ void __launch_bounds__(NTHR, 2) fused_decode(Ptrs P) {
  __shared__ __attribute__((aligned(16))) float lds[L_TOTAL];
  const int j = blockIdx.x, t = threadIdx.x, lane = t & 63;
  const int wave = __builtin_amdgcn_readfirstlane(t >> 6);
  unsigned char* ws = P.ws;
  STAMP(0);
  const u64 tag = *reinterpret_cast<const u64*>(ws + WS_EPOCH) + 1ull;
  const u64 tag48 = (tag & 0xFFFFFFFFFFFFull) << 16;
  const __amdgpu_buffer_rsrc_t rsW = __builtin_amdgcn_make_buffer_rsrc(ws, 0, (int)WS_BYTES, 0x00020000);

  int mod, h, s, k0, k1;
  if (j < 8 * NSA) { mod = 0; h = j / NSA; s = j - h * NSA; k0 = s * PERA; k1 = min(k0 + PERA, WINDOW); k0 += SK - WINDOW; k1 += SK - WINDOW; }
  else { const int jj = j - 8 * NSA; mod = 1; h = jj / NSB; s = jj - h * NSB; k0 = s * PERB; k1 = min(k0 + PERB, SK); }
  const int np = (k1 - k0) >> 1;
  const u16* kbase = (mod ? P.kB : P.kA) + (size_t)h * SK * DH;
  const u16* vbase = (mod ? P.vB : P.vA) + (size_t)h * SK * DH;
  const __amdgpu_buffer_rsrc_t rsK = __builtin_amdgcn_make_buffer_rsrc((void*)kbase, 0, SK * DH * 2, 0x00020000);
  const __amdgpu_buffer_rsrc_t rsV = __builtin_amdgcn_make_buffer_rsrc((void*)vbase, 0, SK * DH * 2, 0x00020000);
  const int voff = (lane >> 5) * 512 + (lane & 31) * 16;
#define KV_SOFF(i) ((k0 + 2 * min(wave + 8 * (i), np - 1)) * 512)

  const int mod1 = j >> 7, col0 = (j & 127) * 32;
  const u16* wq = mod1 ? P.wqB : P.wqA;
  const int seg = t & 3, r0 = t >> 2;
  u32x4 wv[4]; u16 xr[4];
#pragma unroll
  for (int i = 0; i < 4; ++i) {
    const int row = min(r0 + 128 * i, DM - 1);
    wv[i] = __builtin_nontemporal_load(reinterpret_cast<const u32x4*>(wq + (size_t)row * 4096 + col0 + seg * 8));
    xr[i] = P.x[row];
  }
  const int tg = min(t, DM - 1);
  const u16 gxr = P.x[tg], gwr = P.gw[tg];

  u32x4 Kr[NSLOT], Vr[NSLOT];
  if (wave >= 2) {
#pragma unroll
    for (int i = 0; i < EARLY; ++i) Kr[i] = __builtin_amdgcn_raw_buffer_load_b128(rsK, voff, KV_SOFF(i), AUX_NT);
  }
  if (wave == 1) {
    const u32x4 z = {0u, 0u, 0u, 0u};
#pragma unroll
    for (int i = 0; i < 8; ++i) __builtin_amdgcn_raw_buffer_store_b128(z, rsW, (int)(WS_I1 + j * 8192 + (i * 64 + lane) * 16), 0, AUX_SC1);
    if (j < 112) {
#pragma unroll
      for (int i = 0; i < 8; ++i) __builtin_amdgcn_raw_buffer_store_b128(z, rsW, (int)(WS_I2 + j * 8192 + (i * 64 + lane) * 16), 0, AUX_SC1);
    }
  }
  __builtin_amdgcn_sched_barrier(0);

  const float gp = (t < DM) ? rbf(bf2f(gxr) * bf2f(gwr)) : 0.f;
  float xv[4];
#pragma unroll
  for (int i = 0; i < 4; ++i) xv[i] = (r0 + 128 * i < DM) ? bf2f(xr[i]) : 0.f;
  {
    float acc[8];
#pragma unroll
    for (int k = 0; k < 8; ++k) acc[k] = 0.f;
#pragma unroll
    for (int i = 0; i < 4; ++i)
#pragma unroll
      for (int k = 0; k < 4; ++k) { acc[2 * k] = fmaf(xv[i], bflo(wv[i][k]), acc[2 * k]); acc[2 * k + 1] = fmaf(xv[i], bfhi(wv[i][k]), acc[2 * k + 1]); }
#pragma unroll
    for (int k = 0; k < 8; ++k) {
      float v = acc[k];
      v += dpp<ROR4>(v); v += dpp<XOR8>(v);
      v = swap16_add(v, v); v = swap32_add(v, v);
      acc[k] = v;
    }
    if (lane < 4) {
      *reinterpret_cast<f32x4*>(&lds[L_RED1 + wave * 32 + lane * 8]) = f32x4{acc[0], acc[1], acc[2], acc[3]};
      *reinterpret_cast<f32x4*>(&lds[L_RED1 + wave * 32 + lane * 8 + 4]) = f32x4{acc[4], acc[5], acc[6], acc[7]};
    }
    float g = gp;
    g += dpp<XOR1>(g); g += dpp<XOR2>(g); g += dpp<ROR4>(g); g += dpp<XOR8>(g);
    g = swap16_add(g, g); g = swap32_add(g, g);
    if (lane == 0) lds[L_GP + wave] = g;
  }
  lds_barrier();
  STAMP(1);
  if (wave == 0) {
    if (lane < 32) {
      float a = lds[L_RED1 + lane];
#pragma unroll
      for (int w = 1; w < NWAVE; ++w) a += lds[L_RED1 + w * 32 + lane];
      word_store(ws, WS_QW + (mod1 * 4096 + col0 + lane) * 8, tag48 | (u64)f2bf_bits(a));
    }
    STAMP(2);
    u64 qw[8];
    for (unsigned spins = 0; spins < SPIN_CAP; ++spins) {
      bool ok = true;
#pragma unroll
      for (int k = 0; k < 8; ++k) { qw[k] = word_load(ws, WS_QW + (mod * 4096 + h * 512 + lane + 64 * k) * 8); }
      const u64 f0 = word_load(ws, WS_RDY + (mod * 128 + h * 16 + (lane & 15)) * 64);
      const u64 f1 = word_load(ws, WS_RDY + min(lane, 111) * 64);
      const u64 f2 = word_load(ws, WS_RDY + min(lane + 64, 111) * 64);
#pragma unroll
      for (int k = 0; k < 8; ++k) ok &= (qw[k] & ~0xFFFFull) == tag48;
      ok &= (f0 == tag) && (f1 == tag) && (f2 == tag);
      if (__all(ok)) break;
      __builtin_amdgcn_s_sleep(2);
    }
    u16* ql = reinterpret_cast<u16*>(&lds[L_Q]);
#pragma unroll
    for (int k = 0; k < 8; ++k) ql[lane + 64 * k] = (u16)qw[k];
    STAMP(3);
  } else if (wave == 1) {
    drain_vm();
    if (lane == 0) word_store(ws, WS_RDY + j * 64, tag);
  } else if (wave == 2 && lane == 0) {
    float g = 0.f;
#pragma unroll
    for (int w = 0; w < NWAVE; ++w) g += lds[L_GP + w];
    g = rbf(g);
    lds[L_GP + 8] = rbf(1.f / (1.f + __expf(-g)));
  }
  lds_barrier();

  if (wave < 2) {
#pragma unroll
    for (int i = 0; i < EARLY; ++i) Kr[i] = __builtin_amdgcn_raw_buffer_load_b128(rsK, voff, KV_SOFF(i), AUX_NT);
  }
#pragma unroll
  for (int i = EARLY; i < NSLOT; ++i) Kr[i] = __builtin_amdgcn_raw_buffer_load_b128(rsK, voff, KV_SOFF(i), AUX_NT);
#pragma unroll
  for (int i = 0; i < NSLOT; ++i) Vr[i] = __builtin_amdgcn_raw_buffer_load_b128(rsV, voff, KV_SOFF(i), AUX_NT);
  __builtin_amdgcn_sched_barrier(0);
  STAMP(4);

  const int c = lane & 31, half = lane >> 5;
  u32x4 qk[2];
  qk[0] = *reinterpret_cast<const u32x4*>(&lds[L_Q + c * 4]);
  qk[1] = *reinterpret_cast<const u32x4*>(&lds[L_Q + 128 + c * 4]);
  const bool b3 = lane & 8, b2 = lane & 4, b1 = lane & 2;
  const int ulane = ((lane >> 4) & 1) * 4 + ((lane >> 3) & 1) * 2 + ((lane >> 2) & 1);
  constexpr float C2 = 0.0625f * 1.4426950408889634f;
  float tsc[3];
#pragma unroll
  for (int gi = 0; gi < 3; ++gi) {
    float xx[16];
#pragma unroll
    for (int u = 0; u < 8; ++u) {
      const int i = gi * 8 + u;
#pragma unroll
      for (int g = 0; g < 2; ++g) {
        float d = 0.f;
        if (i < NSLOT) {
#pragma unroll
          for (int k = 0; k < 4; ++k) d = dot2(Kr[i][k], qk[g][k], d);
        }
        xx[u * 2 + g] = d;
      }
    }
    float y[8], z[4], w2[2];
#pragma unroll
    for (int v = 0; v < 8; ++v) y[v] = swap16_add(xx[v], xx[v + 8]);
#pragma unroll
    for (int v = 0; v < 4; ++v) { const float keep = b3 ? y[v + 4] : y[v], send = b3 ? y[v] : y[v + 4]; z[v] = keep + dpp<XOR8>(send); }
#pragma unroll
    for (int v = 0; v < 2; ++v) { const float keep = b2 ? z[v + 2] : z[v], send = b2 ? z[v] : z[v + 2]; w2[v] = keep + dpp<XOR7>(send); }
    float sfin = (b1 ? w2[1] : w2[0]) + dpp<XOR2>(b1 ? w2[0] : w2[1]);
    sfin += dpp<XOR1>(sfin);
    const bool valid = (wave + 8 * (gi * 8 + ulane)) < np;
    tsc[gi] = valid ? rbf(sfin) * C2 : -INFINITY;
  }
  float mx = fmaxf(fmaxf(tsc[0], tsc[1]), tsc[2]);
  mx = fmaxf(mx, dpp<ROR4>(mx)); mx = fmaxf(mx, dpp<XOR8>(mx)); mx = x16_max(mx); mx = x32_max(mx);
  float pv[3], ls = 0.f;
#pragma unroll
  for (int gi = 0; gi < 3; ++gi) { pv[gi] = __builtin_amdgcn_exp2f(tsc[gi] - mx); ls += pv[gi]; }
  ls += dpp<ROR4>(ls); ls += dpp<XOR8>(ls); ls = swap16_add(ls, ls); ls = swap32_add(ls, ls);
  {
    float* pb = &lds[L_P + (wave * 2 + half) * 48];
    if (!(lane & 1)) {
#pragma unroll
      for (int gi = 0; gi < 3; ++gi) pb[(gi * 8 + ulane) * 2 + (b1 ? 1 : 0)] = pv[gi];
    }
    if (lane == 0 || lane == 2) { lds[L_ML + wave * 4 + (b1 ? 2 : 0)] = mx; lds[L_ML + wave * 4 + (b1 ? 2 : 0) + 1] = ls; }
  }
  lds_wait();
  STAMP(5);
  float acc[2][8];
#pragma unroll
  for (int g = 0; g < 2; ++g)
#pragma unroll
    for (int k = 0; k < 8; ++k) acc[g][k] = 0.f;
  {
    const float* pb = &lds[L_P + (wave * 2 + half) * 48];
#pragma unroll
    for (int i2 = 0; i2 < (NSLOT + 1) / 2; ++i2) {
      const f32x4 pp = *reinterpret_cast<const f32x4*>(pb + i2 * 4);
#pragma unroll
      for (int e = 0; e < 2; ++e) {
        const int i = 2 * i2 + e;
        if (i < NSLOT) {
          float vf[8];
#pragma unroll
          for (int k = 0; k < 4; ++k) { vf[2 * k] = bflo(Vr[i][k]); vf[2 * k + 1] = bfhi(Vr[i][k]); }
#pragma unroll
          for (int g = 0; g < 2; ++g)
#pragma unroll
            for (int k = 0; k < 8; ++k) acc[g][k] = fmaf(pp[e * 2 + g], vf[k], acc[g][k]);
        }
      }
    }
  }
  {
    float o8[8];
#pragma unroll
    for (int k = 0; k < 8; ++k) o8[k] = swap32_add(acc[0][k], acc[1][k]);
    float* mg = &lds[L_MRG + wave * 512 + half * 256 + c * 8];
    *reinterpret_cast<f32x4*>(mg) = f32x4{o8[0], o8[1], o8[2], o8[3]};
    *reinterpret_cast<f32x4*>(mg + 4) = f32x4{o8[4], o8[5], o8[6], o8[7]};
  }
  lds_barrier();
  STAMP(6);
  {
    const int g = t >> 8, d = t & 255;
    float M = -INFINITY;
#pragma unroll
    for (int w = 0; w < NWAVE; ++w) M = fmaxf(M, lds[L_ML + w * 4 + g * 2]);
    float L = 0.f, o = 0.f;
#pragma unroll
    for (int w = 0; w < NWAVE; ++w) {
      const float wt = __builtin_amdgcn_exp2f(lds[L_ML + w * 4 + g * 2] - M);
      L = fmaf(wt, lds[L_ML + w * 4 + g * 2 + 1], L);
      o = fmaf(wt, lds[L_MRG + w * 512 + g * 256 + d], o);
    }
    const u32 ib = WS_I1 + (mod * 128 + (2 * h + g) * 8 + (d >> 5)) * 8192 + s * 34 * 8;
    word_store(ws, ib + (d & 31) * 8, FULL | (u64)__float_as_uint(o));
    if ((d & 31) == 0) { word_store(ws, ib + 32 * 8, FULL | (u64)__float_as_uint(M)); word_store(ws, ib + 33 * 8, FULL | (u64)__float_as_uint(L)); }
  }
  STAMP(7);

  const int hq3 = (j & 127) >> 3, row3 = (j & 127) * 32;
  const int rg3 = t / 56, cg3 = t - rg3 * 56;
  u32x4 Wo[4];
  {
    const u16* wo = (mod1 ? P.woB : P.woA) + (size_t)row3 * DM;
    const int tr = min(t, 447), rgc = tr / 56, cgc = tr - rgc * 56;
#pragma unroll
    for (int i = 0; i < 4; ++i) Wo[i] = __builtin_nontemporal_load(reinterpret_cast<const u32x4*>(wo + (size_t)(rgc + 8 * i) * DM + cgc * 8));
  }
  const int ns3 = mod1 ? NSB : NSA, nw = 34 * ns3;
  {
    const bool need0 = t < nw, need1 = (t + 512) < nw;
    u64 w0 = 0ull, w1 = 0ull;
    for (unsigned spins = 0; spins < SPIN_CAP; ++spins) {
      if (need0 && !(w0 & FULL)) w0 = word_load(ws, WS_I1 + j * 8192 + t * 8);
      if (need1 && !(w1 & FULL)) w1 = word_load(ws, WS_I1 + j * 8192 + (t + 512) * 8);
      const bool ok = (!need0 || (w0 & FULL)) && (!need1 || (w1 & FULL));
      if (__all(ok)) break;
      __builtin_amdgcn_s_sleep(2);
    }
    if (need0) lds[L_ST1 + t] = __uint_as_float((u32)w0);
    if (need1) lds[L_ST1 + t + 512] = __uint_as_float((u32)w1);
  }
  lds_barrier();
  STAMP(8);
  {
    const float ms = (lane < ns3) ? lds[L_ST1 + lane * 34 + 32] : -INFINITY;
    const float lsv = (lane < ns3) ? lds[L_ST1 + lane * 34 + 33] : 0.f;
    float M = ms;
    M = fmaxf(M, dpp<XOR1>(M)); M = fmaxf(M, dpp<XOR2>(M)); M = fmaxf(M, dpp<ROR4>(M)); M = fmaxf(M, dpp<XOR8>(M)); M = x16_max(M); M = x32_max(M);
    const float wt = (lane < ns3) ? __builtin_amdgcn_exp2f(ms - M) : 0.f;
    float L = wt * lsv;
    L += dpp<XOR1>(L); L += dpp<XOR2>(L); L += dpp<ROR4>(L); L += dpp<XOR8>(L); L = swap16_add(L, L); L = swap32_add(L, L);
    const float cw = wt / L;
    const int dd = lane & 31, hs = lane >> 5;
    const int sA = wave + 8 * hs, sB = wave + 16 + 8 * hs;
    const float cA = __shfl(cw, sA, 64), cB = __shfl(cw, sB, 64);
    float v = (sA < ns3 ? cA * lds[L_ST1 + sA * 34 + dd] : 0.f) + (sB < ns3 ? cB * lds[L_ST1 + sB * 34 + dd] : 0.f);
    v = swap32_add(v, v);
    if (lane < 32) lds[L_RED2 + wave * 32 + dd] = v;
  }
  lds_barrier();
  STAMP(9);
  if (t < 448) {
    float pacc[8];
#pragma unroll
    for (int k = 0; k < 8; ++k) pacc[k] = 0.f;
#pragma unroll
    for (int i = 0; i < 4; ++i) {
      const int row = rg3 + 8 * i;
      float ov = 0.f;
#pragma unroll
      for (int w = 0; w < NWAVE; ++w) ov += lds[L_RED2 + w * 32 + row];
      ov = rbf(ov);
#pragma unroll
      for (int k = 0; k < 4; ++k) { pacc[2 * k] = fmaf(ov, bflo(Wo[i][k]), pacc[2 * k]); pacc[2 * k + 1] = fmaf(ov, bfhi(Wo[i][k]), pacc[2 * k + 1]); }
    }
    float* r3 = &lds[L_RED3 + rg3 * 448 + cg3 * 8];
    *reinterpret_cast<f32x4*>(r3) = f32x4{pacc[0], pacc[1], pacc[2], pacc[3]};
    *reinterpret_cast<f32x4*>(r3 + 4) = f32x4{pacc[4], pacc[5], pacc[6], pacc[7]};
  }
  lds_barrier();
  STAMP(10);
  if (t < 448) {
    float sacc = lds[L_RED3 + t];
#pragma unroll
    for (int rg = 1; rg < 8; ++rg) sacc += lds[L_RED3 + rg * 448 + t];
    word_store(ws, WS_I2 + (t >> 2) * 8192 + (j * 4 + (t & 3)) * 8, FULL | (u64)__float_as_uint(sacc));
  }
  STAMP(11);

  if (j < 112) {
    f32x4 v = {0.f, 0.f, 0.f, 0.f};
    if (t < 256) {
      u64 w[4] = {0ull, 0ull, 0ull, 0ull};
      for (unsigned spins = 0; spins < SPIN_CAP; ++spins) {
#pragma unroll
        for (int k = 0; k < 4; ++k) if (!(w[k] & FULL)) w[k] = word_load(ws, WS_I2 + j * 8192 + (t * 4 + k) * 8);
        const bool ok = (w[0] & w[1] & w[2] & w[3] & FULL) != 0ull;
        if (__all(ok)) break;
        __builtin_amdgcn_s_sleep(2);
      }
#pragma unroll
      for (int k = 0; k < 4; ++k) {
        float q = __uint_as_float((u32)w[k]);
        q += dpp<XOR1>(q); q += dpp<XOR2>(q); q += dpp<ROR4>(q); q += dpp<XOR8>(q); q = swap16_add(q, q); q = swap32_add(q, q);
        v[k] = q;
      }
      if (lane == 0) *reinterpret_cast<f32x4*>(&lds[L_FIN + wave * 4]) = v;
    }
    lds_barrier();
    STAMP(12);
    if (t == 0) {
      const float g = lds[L_GP + 8], og = rbf(1.f - g);
      u32 o[4];
#pragma unroll
      for (int k = 0; k < 4; ++k) {
        const float a = rbf(lds[L_FIN + k] + lds[L_FIN + 4 + k]), b = rbf(lds[L_FIN + 8 + k] + lds[L_FIN + 12 + k]);
        o[k] = f2bf_bits(rbf(g * a) + rbf(og * b));
      }
      *reinterpret_cast<u32x2*>(P.out + j * 4) = u32x2{o[0] | (o[1] << 16), o[2] | (o[3] << 16)};
      if (j == 0) *reinterpret_cast<u64*>(ws + WS_EPOCH) = tag;
    }
  }
  STAMP(13);
}
}

extern "C" void kernel_launch(void* const* d_in, const int* in_sizes, int n_in, void* d_out, int out_size, void* d_ws, size_t ws_size,
                              hipStream_t stream) {
  Ptrs P;
  P.x = (const u16*)d_in[0]; P.kA = (const u16*)d_in[1]; P.vA = (const u16*)d_in[2]; P.wqA = (const u16*)d_in[3]; P.woA = (const u16*)d_in[4];
  P.kB = (const u16*)d_in[6]; P.vB = (const u16*)d_in[7]; P.wqB = (const u16*)d_in[8]; P.woB = (const u16*)d_in[9]; P.gw = (const u16*)d_in[10];
  P.out = (u16*)d_out; P.ws = (unsigned char*)d_ws;
  hipLaunchKernelGGL(fused_decode, dim3(256), dim3(NTHR), 0, stream, P);
}
```

```cpp
#include <hip/hip_runtime.h>
#include <hip/hip_bf16.h>
#include <stdint.h>

namespace {
typedef unsigned short u16;
typedef unsigned int u32;
typedef unsigned long long u64;
typedef __attribute__((ext_vector_type(4))) unsigned u32x4;
typedef __attribute__((ext_vector_type(2))) unsigned u32x2;
typedef __attribute__((ext_vector_type(4))) float f32x4;
typedef __attribute__((ext_vector_type(2))) __bf16 bf16x2;

constexpr int DM = 448, SK = 8192, DH = 256, WINDOW = 2048;
constexpr int NSA = 7, NSB = 25;
constexpr int PERA = 294, PERB = 328;
constexpr int NTHR = 512, NWAVE = 8;
constexpr int NSLOT = 21;
#define EARLY_K 4
constexpr int EARLY = EARLY_K;

constexpr u32 WS_EPOCH = 0;
constexpr u32 WS_RDY = 0x1000;
constexpr u32 WS_QW = 0x10000;
constexpr u32 WS_I1 = 0x100000;
constexpr u32 WS_I2 = 0x300000;
constexpr u32 WS_BYTES = 0x400000;
constexpr int AUX_SC1 = 16;
#define KV_AUX 2
constexpr int AUX_NT = KV_AUX;
constexpr unsigned SPIN_CAP = 1u << 17;
constexpr u64 FULL = 1ull << 32;

constexpr int L_RED1 = 0;
constexpr int L_GP = 512;
constexpr int L_Q = 544;
constexpr int L_P = 800;
constexpr int L_ML = 1568;
constexpr int L_MRG = 1600;
constexpr int L_ST1 = L_MRG + 4096;
constexpr int L_RED2 = L_ST1 + 1024;
constexpr int L_RED3 = L_RED2 + 256;
constexpr int L_FIN = L_RED3 + 3584;
constexpr int L_TOTAL = L_FIN + 16;

__device__ __forceinline__ float bf2f(u16 h) { return __uint_as_float(((u32)h) << 16); }
__device__ __forceinline__ float bflo(u32 w) { return __uint_as_float(w << 16); }
__device__ __forceinline__ float bfhi(u32 w) { return __uint_as_float(w & 0xffff0000u); }
__device__ __forceinline__ u32 f2bf_bits(float f) { return (u32)__builtin_bit_cast(u16, (__bf16)f); }
__device__ __forceinline__ float rbf(float f) { return __uint_as_float(f2bf_bits(f) << 16); }
__device__ __forceinline__ u32 pack2(float lo, float hi) { return f2bf_bits(lo) | (f2bf_bits(hi) << 16); }
__device__ __forceinline__ float dot2(u32 a, u32 b, float c) {
  return __builtin_amdgcn_fdot2_f32_bf16(__builtin_bit_cast(bf16x2, a), __builtin_bit_cast(bf16x2, b), c, false);
}
template <int CTRL> __device__ __forceinline__ float dpp(float x) {
  return __builtin_bit_cast(float, __builtin_amdgcn_mov_dpp(__builtin_bit_cast(int, x), CTRL, 0xf, 0xf, true));
}
constexpr int XOR1 = 0xB1, XOR2 = 0x4E, XOR7 = 0x141, XOR8 = 0x128, ROR4 = 0x124;
__device__ __forceinline__ float swap16_add(float a, float b) {
  auto s = __builtin_amdgcn_permlane16_swap(__float_as_uint(a), __float_as_uint(b), false, false);
  return __uint_as_float(s[0]) + __uint_as_float(s[1]);
}
__device__ __forceinline__ float swap32_add(float a, float b) {
  auto s = __builtin_amdgcn_permlane32_swap(__float_as_uint(a), __float_as_uint(b), false, false);
  return __uint_as_float(s[0]) + __uint_as_float(s[1]);
}
__device__ __forceinline__ float x16_max(float x) {
  auto s = __builtin_amdgcn_permlane16_swap(__float_as_uint(x), __float_as_uint(x), false, false);
  return fmaxf(__uint_as_float(s[0]), __uint_as_float(s[1]));
}
__device__ __forceinline__ float x32_max(float x) {
  auto s = __builtin_amdgcn_permlane32_swap(__float_as_uint(x), __float_as_uint(x), false, false);
  return fmaxf(__uint_as_float(s[0]), __uint_as_float(s[1]));
}
__device__ __forceinline__ void lds_barrier() { asm volatile("s_waitcnt lgkmcnt(0)\n\ts_barrier" ::: "memory"); }
__device__ __forceinline__ void lds_wait() { asm volatile("s_waitcnt lgkmcnt(0)" ::: "memory"); }
__device__ __forceinline__ void drain_vm() { asm volatile("s_waitcnt vmcnt(0)" ::: "memory"); }
__device__ __forceinline__ void word_store(unsigned char* ws, u32 off, u64 v) {
  __hip_atomic_store(reinterpret_cast<u64*>(ws + off), v, __ATOMIC_RELAXED, __HIP_MEMORY_SCOPE_AGENT);
}
__device__ __forceinline__ u64 word_load(unsigned char* ws, u32 off) {
  return __hip_atomic_load(reinterpret_cast<u64*>(ws + off), __ATOMIC_RELAXED, __HIP_MEMORY_SCOPE_AGENT);
}

#define STAMP(k) do {} while (0)

struct Ptrs {
  const u16* x; const u16* kA; const u16* vA; const u16* wqA; const u16* woA;
  const u16* kB; const u16* vB; const u16* wqB; const u16* woB; const u16* gw;
  u16* out; unsigned char* ws;
};

__global__ void __launch_bounds__(NTHR, 2) fused_decode(Ptrs P) {
  __shared__ __attribute__((aligned(16))) float lds[L_TOTAL];
  const int j = blockIdx.x, t = threadIdx.x, lane = t & 63;
  const int wave = __builtin_amdgcn_readfirstlane(t >> 6);
  unsigned char* ws = P.ws;
  STAMP(0);
  const u64 tag = *reinterpret_cast<const u64*>(ws + WS_EPOCH) + 1ull;
  const u64 tag48 = (tag & 0xFFFFFFFFFFFFull) << 16;
  const __amdgpu_buffer_rsrc_t rsW = __builtin_amdgcn_make_buffer_rsrc(ws, 0, (int)WS_BYTES, 0x00020000);

  int mod, h, s, k0, k1;
  if (j < 8 * NSA) { mod = 0; h = j / NSA; s = j - h * NSA; k0 = s * PERA; k1 = min(k0 + PERA, WINDOW); k0 += SK - WINDOW; k1 += SK - WINDOW; }
  else { const int jj = j - 8 * NSA; mod = 1; h = jj / NSB; s = jj - h * NSB; k0 = s * PERB; k1 = min(k0 + PERB, SK); }
#define INTERLEAVE 0
  const int nsp = mod ? NSB : NSA, kfirst = mod ? 0 : SK - WINDOW, npairs = mod ? SK / 2 : WINDOW / 2;
  const int np = INTERLEAVE ? npairs : (k1 - k0) >> 1;
#define PAIR_OF(i) (INTERLEAVE ? ((wave + 8 * (i)) * nsp + s) : (wave + 8 * (i)))
  const u16* kbase = (mod ? P.kB : P.kA) + (size_t)h * SK * DH;
  const u16* vbase = (mod ? P.vB : P.vA) + (size_t)h * SK * DH;
  const __amdgpu_buffer_rsrc_t rsK = __builtin_amdgcn_make_buffer_rsrc((void*)kbase, 0, SK * DH * 2, 0x00020000);
  const __amdgpu_buffer_rsrc_t rsV = __builtin_amdgcn_make_buffer_rsrc((void*)vbase, 0, SK * DH * 2, 0x00020000);
  const int voff = (lane >> 5) * 512 + (lane & 31) * 16;
#define KV_SOFF(i) (((INTERLEAVE ? kfirst : k0) + 2 * min(PAIR_OF(i), np - 1)) * 512)

  const bool qprod = !(j & 1);
  const int pj = j >> 1, mod1 = pj >> 6, col0 = (pj & 63) * 64;
  const u16* wq = mod1 ? P.wqB : P.wqA;
  const int seg = t & 7, r0 = t >> 3;
  u32x4 wv[7]; u16 xr[7];
  if (qprod) {
#pragma unroll
    for (int i = 0; i < 7; ++i) {
      const int row = r0 + 64 * i;
      wv[i] = __builtin_nontemporal_load(reinterpret_cast<const u32x4*>(wq + (size_t)row * 4096 + col0 + seg * 8));
      xr[i] = P.x[row];
    }
  }
  const int tg = min(t, DM - 1);
  const u16 gxr = P.x[tg], gwr = P.gw[tg];

  u32x4 Kr[NSLOT], Vr[NSLOT];
  if (wave >= 2) {
#pragma unroll
    for (int i = 0; i < EARLY; ++i) Kr[i] = __builtin_amdgcn_raw_buffer_load_b128(rsK, voff, KV_SOFF(i), AUX_NT);
  }
  if (wave == 1) {
    const u32x4 z = {0u, 0u, 0u, 0u};
#pragma unroll
    for (int i = 0; i < 8; ++i) __builtin_amdgcn_raw_buffer_store_b128(z, rsW, (int)(WS_I1 + j * 8192 + (i * 64 + lane) * 16), 0, AUX_SC1);
    if (j < 112) {
#pragma unroll
      for (int i = 0; i < 8; ++i) __builtin_amdgcn_raw_buffer_store_b128(z, rsW, (int)(WS_I2 + j * 8192 + (i * 64 + lane) * 16), 0, AUX_SC1);
    }
  }
  __builtin_amdgcn_sched_barrier(0);

  const float gp = (t < DM) ? rbf(bf2f(gxr) * bf2f(gwr)) : 0.f;
  {
    if (qprod) {
      float acc[8];
#pragma unroll
      for (int k = 0; k < 8; ++k) acc[k] = 0.f;
#pragma unroll
      for (int i = 0; i < 7; ++i) {
        const float xv = bf2f(xr[i]);
#pragma unroll
        for (int k = 0; k < 4; ++k) { acc[2 * k] = fmaf(xv, bflo(wv[i][k]), acc[2 * k]); acc[2 * k + 1] = fmaf(xv, bfhi(wv[i][k]), acc[2 * k + 1]); }
      }
#pragma unroll
      for (int k = 0; k < 8; ++k) {
        float v = acc[k];
        v += dpp<XOR8>(v);
        v = swap16_add(v, v); v = swap32_add(v, v);
        acc[k] = v;
      }
      if (lane < 8) {
        *reinterpret_cast<f32x4*>(&lds[L_RED1 + wave * 64 + lane * 8]) = f32x4{acc[0], acc[1], acc[2], acc[3]};
        *reinterpret_cast<f32x4*>(&lds[L_RED1 + wave * 64 + lane * 8 + 4]) = f32x4{acc[4], acc[5], acc[6], acc[7]};
      }
    }
    float g = gp;
    g += dpp<XOR1>(g); g += dpp<XOR2>(g); g += dpp<ROR4>(g); g += dpp<XOR8>(g);
    g = swap16_add(g, g); g = swap32_add(g, g);
    if (lane == 0) lds[L_GP + wave] = g;
  }
  lds_barrier();
  STAMP(1);
  if (wave == 0) {
    if (qprod) {
      float a = lds[L_RED1 + lane];
#pragma unroll
      for (int w = 1; w < NWAVE; ++w) a += lds[L_RED1 + w * 64 + lane];
      word_store(ws, WS_QW + (mod1 * 4096 + col0 + lane) * 8, tag48 | (u64)f2bf_bits(a));
    }
    STAMP(2);
    u64 qw[8];
    for (unsigned spins = 0; spins < SPIN_CAP; ++spins) {
      bool ok = true;
#pragma unroll
      for (int k = 0; k < 8; ++k) { qw[k] = word_load(ws, WS_QW + (mod * 4096 + h * 512 + lane + 64 * k) * 8); }
      const u64 f0 = word_load(ws, WS_RDY + (mod * 128 + h * 16 + (lane & 15)) * 64);
      const u64 f1 = word_load(ws, WS_RDY + min(lane, 111) * 64);
      const u64 f2 = word_load(ws, WS_RDY + min(lane + 64, 111) * 64);
#pragma unroll
      for (int k = 0; k < 8; ++k) ok &= (qw[k] & ~0xFFFFull) == tag48;
      ok &= (f0 == tag) && (f1 == tag) && (f2 == tag);
      if (__all(ok)) break;
      __builtin_amdgcn_s_sleep(2);
    }
    u16* ql = reinterpret_cast<u16*>(&lds[L_Q]);
#pragma unroll
    for (int k = 0; k < 8; ++k) ql[lane + 64 * k] = (u16)qw[k];
    STAMP(3);
  } else if (wave == 1) {
    drain_vm();
    if (lane == 0) word_store(ws, WS_RDY + j * 64, tag);
  } else if (wave == 2 && lane == 0) {
    float g = 0.f;
#pragma unroll
    for (int w = 0; w < NWAVE; ++w) g += lds[L_GP + w];
    g = rbf(g);
    lds[L_GP + 8] = rbf(1.f / (1.f + __expf(-g)));
  }
  lds_barrier();

  if (wave < 2) {
#pragma unroll
    for (int i = 0; i < EARLY; ++i) Kr[i] = __builtin_amdgcn_raw_buffer_load_b128(rsK, voff, KV_SOFF(i), AUX_NT);
  }
#pragma unroll
  for (int i = EARLY; i < NSLOT; ++i) Kr[i] = __builtin_amdgcn_raw_buffer_load_b128(rsK, voff, KV_SOFF(i), AUX_NT);
#pragma unroll
  for (int i = 0; i < NSLOT; ++i) Vr[i] = __builtin_amdgcn_raw_buffer_load_b128(rsV, voff, KV_SOFF(i), AUX_NT);
  __builtin_amdgcn_sched_barrier(0);
  STAMP(4);

  const int c = lane & 31, half = lane >> 5;
  u32x4 qk[2];
  qk[0] = *reinterpret_cast<const u32x4*>(&lds[L_Q + c * 4]);
  qk[1] = *reinterpret_cast<const u32x4*>(&lds[L_Q + 128 + c * 4]);
  const bool b3 = lane & 8, b2 = lane & 4, b1 = lane & 2;
  const int ulane = ((lane >> 4) & 1) * 4 + ((lane >> 3) & 1) * 2 + ((lane >> 2) & 1);
  constexpr float C2 = 0.0625f * 1.4426950408889634f;
  float tsc[3];
#pragma unroll
  for (int gi = 0; gi < 3; ++gi) {
    float xx[16];
#pragma unroll
    for (int u = 0; u < 8; ++u) {
      const int i = gi * 8 + u;
#pragma unroll
      for (int g = 0; g < 2; ++g) {
        float d = 0.f;
        if (i < NSLOT) {
#pragma unroll
          for (int k = 0; k < 4; ++k) d = dot2(Kr[i][k], qk[g][k], d);
        }
        xx[u * 2 + g] = d;
      }
    }
    float y[8], z[4], w2[2];
#pragma unroll
    for (int v = 0; v < 8; ++v) y[v] = swap16_add(xx[v], xx[v + 8]);
#pragma unroll
    for (int v = 0; v < 4; ++v) { const float keep = b3 ? y[v + 4] : y[v], send = b3 ? y[v] : y[v + 4]; z[v] = keep + dpp<XOR8>(send); }
#pragma unroll
    for (int v = 0; v < 2; ++v) { const float keep = b2 ? z[v + 2] : z[v], send = b2 ? z[v] : z[v + 2]; w2[v] = keep + dpp<XOR7>(send); }
    float sfin = (b1 ? w2[1] : w2[0]) + dpp<XOR2>(b1 ? w2[0] : w2[1]);
    sfin += dpp<XOR1>(sfin);
    const bool valid = (INTERLEAVE ? ((wave + 8 * (gi * 8 + ulane)) * nsp + s) : (wave + 8 * (gi * 8 + ulane))) < np;
    tsc[gi] = valid ? rbf(sfin) * C2 : -INFINITY;
  }
  float mx = fmaxf(fmaxf(tsc[0], tsc[1]), tsc[2]);
  mx = fmaxf(mx, dpp<ROR4>(mx)); mx = fmaxf(mx, dpp<XOR8>(mx)); mx = x16_max(mx); mx = x32_max(mx);
  float pv[3], ls = 0.f;
#pragma unroll
  for (int gi = 0; gi < 3; ++gi) { pv[gi] = __builtin_amdgcn_exp2f(tsc[gi] - mx); ls += pv[gi]; }
  ls += dpp<ROR4>(ls); ls += dpp<XOR8>(ls); ls = swap16_add(ls, ls); ls = swap32_add(ls, ls);
  {
    float* pb = &lds[L_P + (wave * 2 + half) * 48];
    if (!(lane & 1)) {
#pragma unroll
      for (int gi = 0; gi < 3; ++gi) pb[(gi * 8 + ulane) * 2 + (b1 ? 1 : 0)] = pv[gi];
    }
    if (lane == 0 || lane == 2) { lds[L_ML + wave * 4 + (b1 ? 2 : 0)] = mx; lds[L_ML + wave * 4 + (b1 ? 2 : 0) + 1] = ls; }
  }
  lds_wait();
  STAMP(5);
  float acc[2][8];
#pragma unroll
  for (int g = 0; g < 2; ++g)
#pragma unroll
    for (int k = 0; k < 8; ++k) acc[g][k] = 0.f;
  {
    const float* pb = &lds[L_P + (wave * 2 + half) * 48];
#pragma unroll
    for (int i2 = 0; i2 < (NSLOT + 1) / 2; ++i2) {
      const f32x4 pp = *reinterpret_cast<const f32x4*>(pb + i2 * 4);
#pragma unroll
      for (int e = 0; e < 2; ++e) {
        const int i = 2 * i2 + e;
        if (i < NSLOT) {
          float vf[8];
#pragma unroll
          for (int k = 0; k < 4; ++k) { vf[2 * k] = bflo(Vr[i][k]); vf[2 * k + 1] = bfhi(Vr[i][k]); }
#pragma unroll
          for (int g = 0; g < 2; ++g)
#pragma unroll
            for (int k = 0; k < 8; ++k) acc[g][k] = fmaf(pp[e * 2 + g], vf[k], acc[g][k]);
        }
      }
    }
  }
  {
    float o8[8];
#pragma unroll
    for (int k = 0; k < 8; ++k) o8[k] = swap32_add(acc[0][k], acc[1][k]);
    float* mg = &lds[L_MRG + wave * 512 + half * 256 + c * 8];
    *reinterpret_cast<f32x4*>(mg) = f32x4{o8[0], o8[1], o8[2], o8[3]};
    *reinterpret_cast<f32x4*>(mg + 4) = f32x4{o8[4], o8[5], o8[6], o8[7]};
  }
  lds_barrier();
  STAMP(6);
  {
    const int g = t >> 8, d = t & 255;
    float M = -INFINITY;
#pragma unroll
    for (int w = 0; w < NWAVE; ++w) M = fmaxf(M, lds[L_ML + w * 4 + g * 2]);
    float L = 0.f, o = 0.f;
#pragma unroll
    for (int w = 0; w < NWAVE; ++w) {
      const float wt = __builtin_amdgcn_exp2f(lds[L_ML + w * 4 + g * 2] - M);
      L = fmaf(wt, lds[L_ML + w * 4 + g * 2 + 1], L);
      o = fmaf(wt, lds[L_MRG + w * 512 + g * 256 + d], o);
    }
    const u32 ib = WS_I1 + (mod * 128 + (2 * h + g) * 8 + (d >> 5)) * 8192 + s * 34 * 8;
    word_store(ws, ib + (d & 31) * 8, FULL | (u64)__float_as_uint(o));
    if ((d & 31) == 0) { word_store(ws, ib + 32 * 8, FULL | (u64)__float_as_uint(M)); word_store(ws, ib + 33 * 8, FULL | (u64)__float_as_uint(L)); }
  }
  STAMP(7);

  const int mod3 = j >> 7, hq3 = (j & 127) >> 3, row3 = (j & 127) * 32;
  const int rg3 = t / 56, cg3 = t - rg3 * 56;
  u32x4 Wo[4];
  {
    const u16* wo = (mod3 ? P.woB : P.woA) + (size_t)row3 * DM;
    const int tr = min(t, 447), rgc = tr / 56, cgc = tr - rgc * 56;
#pragma unroll
    for (int i = 0; i < 4; ++i) Wo[i] = __builtin_nontemporal_load(reinterpret_cast<const u32x4*>(wo + (size_t)(rgc + 8 * i) * DM + cgc * 8));
  }
  const int ns3 = mod3 ? NSB : NSA;
  {
    const int dd = lane & 31, hs = lane >> 5;
    const int sA = wave + 8 * hs, sB = wave + 16 + 8 * hs;
    const bool nA = sA < ns3, nB = sB < ns3, nM = lane < ns3;
    const u32 ib = WS_I1 + j * 8192;
    u64 wA = 0ull, wB = 0ull, wM = 0ull, wL = 0ull;
    for (unsigned spins = 0; spins < SPIN_CAP; ++spins) {
      if (nA && !(wA & FULL)) wA = word_load(ws, ib + (sA * 34 + dd) * 8);
      if (nB && !(wB & FULL)) wB = word_load(ws, ib + (sB * 34 + dd) * 8);
      if (nM && !(wM & FULL)) wM = word_load(ws, ib + (lane * 34 + 32) * 8);
      if (nM && !(wL & FULL)) wL = word_load(ws, ib + (lane * 34 + 33) * 8);
      const bool ok = (!nA || (wA & FULL)) && (!nB || (wB & FULL)) && (!nM || ((wM & wL) & FULL));
      if (__all(ok)) break;
      __builtin_amdgcn_s_sleep(1);
    }
    STAMP(8);
    const float ms = nM ? __uint_as_float((u32)wM) : -INFINITY;
    const float lsv = nM ? __uint_as_float((u32)wL) : 0.f;
    float M = ms;
    M = fmaxf(M, dpp<XOR1>(M)); M = fmaxf(M, dpp<XOR2>(M)); M = fmaxf(M, dpp<ROR4>(M)); M = fmaxf(M, dpp<XOR8>(M)); M = x16_max(M); M = x32_max(M);
    const float wt = nM ? __builtin_amdgcn_exp2f(ms - M) : 0.f;
    float L = wt * lsv;
    L += dpp<XOR1>(L); L += dpp<XOR2>(L); L += dpp<ROR4>(L); L += dpp<XOR8>(L); L = swap16_add(L, L); L = swap32_add(L, L);
    const float cw = wt / L;
    const float cA = __shfl(cw, sA, 64), cB = __shfl(cw, sB, 64);
    float v = (nA ? cA * __uint_as_float((u32)wA) : 0.f) + (nB ? cB * __uint_as_float((u32)wB) : 0.f);
    v = swap32_add(v, v);
    if (lane < 32) lds[L_RED2 + wave * 32 + dd] = v;
  }
  lds_barrier();
  STAMP(9);
  if (t < 448) {
    float pacc[8];
#pragma unroll
    for (int k = 0; k < 8; ++k) pacc[k] = 0.f;
#pragma unroll
    for (int i = 0; i < 4; ++i) {
      const int row = rg3 + 8 * i;
      float ov = 0.f;
#pragma unroll
      for (int w = 0; w < NWAVE; ++w) ov += lds[L_RED2 + w * 32 + row];
      ov = rbf(ov);
#pragma unroll
      for (int k = 0; k < 4; ++k) { pacc[2 * k] = fmaf(ov, bflo(Wo[i][k]), pacc[2 * k]); pacc[2 * k + 1] = fmaf(ov, bfhi(Wo[i][k]), pacc[2 * k + 1]); }
    }
    float* r3 = &lds[L_RED3 + rg3 * 448 + cg3 * 8];
    *reinterpret_cast<f32x4*>(r3) = f32x4{pacc[0], pacc[1], pacc[2], pacc[3]};
    *reinterpret_cast<f32x4*>(r3 + 4) = f32x4{pacc[4], pacc[5], pacc[6], pacc[7]};
  }
  lds_barrier();
  STAMP(10);
  if (t < 448) {
    float sacc = lds[L_RED3 + t];
#pragma unroll
    for (int rg = 1; rg < 8; ++rg) sacc += lds[L_RED3 + rg * 448 + t];
    word_store(ws, WS_I2 + (t >> 2) * 8192 + (j * 4 + (t & 3)) * 8, FULL | (u64)__float_as_uint(sacc));
  }
  STAMP(11);

  if (j < 112) {
    f32x4 v = {0.f, 0.f, 0.f, 0.f};
    if (t < 256) {
      u64 w[4] = {0ull, 0ull, 0ull, 0ull};
      for (unsigned spins = 0; spins < SPIN_CAP; ++spins) {
#pragma unroll
        for (int k = 0; k < 4; ++k) if (!(w[k] & FULL)) w[k] = word_load(ws, WS_I2 + j * 8192 + (t * 4 + k) * 8);
        const bool ok = (w[0] & w[1] & w[2] & w[3] & FULL) != 0ull;
        if (__all(ok)) break;
        __builtin_amdgcn_s_sleep(2);
      }
#pragma unroll
      for (int k = 0; k < 4; ++k) {
        float q = __uint_as_float((u32)w[k]);
        q += dpp<XOR1>(q); q += dpp<XOR2>(q); q += dpp<ROR4>(q); q += dpp<XOR8>(q); q = swap16_add(q, q); q = swap32_add(q, q);
        v[k] = q;
      }
      if (lane == 0) *reinterpret_cast<f32x4*>(&lds[L_FIN + wave * 4]) = v;
    }
    lds_barrier();
    STAMP(12);
    if (t == 0) {
      const float g = lds[L_GP + 8], og = rbf(1.f - g);
      u32 o[4];
#pragma unroll
      for (int k = 0; k < 4; ++k) {
        const float a = rbf(lds[L_FIN + k] + lds[L_FIN + 4 + k]), b = rbf(lds[L_FIN + 8 + k] + lds[L_FIN + 12 + k]);
        o[k] = f2bf_bits(rbf(g * a) + rbf(og * b));
      }
      *reinterpret_cast<u32x2*>(P.out + j * 4) = u32x2{o[0] | (o[1] << 16), o[2] | (o[3] << 16)};
      if (j == 0) *reinterpret_cast<u64*>(ws + WS_EPOCH) = tag;
    }
  }
  STAMP(13);
}
}

extern "C" void kernel_launch(void* const* d_in, const int* in_sizes, int n_in, void* d_out, int out_size, void* d_ws, size_t ws_size,
                              hipStream_t stream) {
  Ptrs P;
  P.x = (const u16*)d_in[0]; P.kA = (const u16*)d_in[1]; P.vA = (const u16*)d_in[2]; P.wqA = (const u16*)d_in[3]; P.woA = (const u16*)d_in[4];
  P.kB = (const u16*)d_in[6]; P.vB = (const u16*)d_in[7]; P.wqB = (const u16*)d_in[8]; P.woB = (const u16*)d_in[9]; P.gw = (const u16*)d_in[10];
  P.out = (u16*)d_out; P.ws = (unsigned char*)d_ws;
  hipLaunchKernelGGL(fused_decode, dim3(256), dim3(NTHR), 0, stream, P);
}
```

```cpp
#include <hip/hip_runtime.h>
#include <hip/hip_bf16.h>
#include <stdint.h>

namespace {
typedef unsigned short u16;
typedef unsigned int u32;
typedef unsigned long long u64;
typedef __attribute__((ext_vector_type(4))) unsigned u32x4;
typedef __attribute__((ext_vector_type(2))) unsigned u32x2;
typedef __attribute__((ext_vector_type(4))) float f32x4;
typedef __attribute__((ext_vector_type(2))) __bf16 bf16x2;

constexpr int DM = 448, SK = 8192, DH = 256, WINDOW = 2048;
constexpr int NSA = 7, NSB = 25;
constexpr int PERA = 294, PERB = 328;
constexpr int NTHR = 512, NWAVE = 8;
constexpr int NSLOT = 21;
#define EARLY_K 0
constexpr int EARLY = EARLY_K;
#define MID_K 14
constexpr int MID = MID_K;
#define EODD_K 0
constexpr int EODD = EODD_K;
#define MID_V 0
constexpr int MIDV = MID_V;

constexpr u32 WS_EPOCH = 0;
constexpr u32 WS_RDY = 0x1000;
constexpr u32 WS_QW = 0x10000;
constexpr u32 WS_I1 = 0x100000;
constexpr u32 WS_I2 = 0x300000;
constexpr u32 WS_BYTES = 0x400000;
constexpr int AUX_SC1 = 16;
#define KV_AUX 2
constexpr int AUX_NT = KV_AUX;
constexpr unsigned SPIN_CAP = 1u << 17;
constexpr u64 FULL = 1ull << 32;

constexpr int L_RED1 = 0;
constexpr int L_GP = 512;
constexpr int L_Q = 544;
constexpr int L_P = 800;
constexpr int L_ML = 1568;
constexpr int L_MRG = 1600;
constexpr int L_ST1 = L_MRG + 4096;
constexpr int L_RED2 = L_ST1 + 1024;
constexpr int L_RED3 = L_RED2 + 256;
constexpr int L_FIN = L_RED3 + 3584;
constexpr int L_TOTAL = L_FIN + 16;

__device__ __forceinline__ float bf2f(u16 h) { return __uint_as_float(((u32)h) << 16); }
__device__ __forceinline__ float bflo(u32 w) { return __uint_as_float(w << 16); }
__device__ __forceinline__ float bfhi(u32 w) { return __uint_as_float(w & 0xffff0000u); }
__device__ __forceinline__ u32 f2bf_bits(float f) { return (u32)__builtin_bit_cast(u16, (__bf16)f); }
__device__ __forceinline__ float rbf(float f) { return __uint_as_float(f2bf_bits(f) << 16); }
__device__ __forceinline__ u32 pack2(float lo, float hi) { return f2bf_bits(lo) | (f2bf_bits(hi) << 16); }
__device__ __forceinline__ float dot2(u32 a, u32 b, float c) {
  return __builtin_amdgcn_fdot2_f32_bf16(__builtin_bit_cast(bf16x2, a), __builtin_bit_cast(bf16x2, b), c, false);
}
template <int CTRL> __device__ __forceinline__ float dpp(float x) {
  return __builtin_bit_cast(float, __builtin_amdgcn_mov_dpp(__builtin_bit_cast(int, x), CTRL, 0xf, 0xf, true));
}
constexpr int XOR1 = 0xB1, XOR2 = 0x4E, XOR7 = 0x141, XOR8 = 0x128, ROR4 = 0x124;
__device__ __forceinline__ float swap16_add(float a, float b) {
  auto s = __builtin_amdgcn_permlane16_swap(__float_as_uint(a), __float_as_uint(b), false, false);
  return __uint_as_float(s[0]) + __uint_as_float(s[1]);
}
__device__ __forceinline__ float swap32_add(float a, float b) {
  auto s = __builtin_amdgcn_permlane32_swap(__float_as_uint(a), __float_as_uint(b), false, false);
  return __uint_as_float(s[0]) + __uint_as_float(s[1]);
}
__device__ __forceinline__ float x16_max(float x) {
  auto s = __builtin_amdgcn_permlane16_swap(__float_as_uint(x), __float_as_uint(x), false, false);
  return fmaxf(__uint_as_float(s[0]), __uint_as_float(s[1]));
}
__device__ __forceinline__ float x32_max(float x) {
  auto s = __builtin_amdgcn_permlane32_swap(__float_as_uint(x), __float_as_uint(x), false, false);
  return fmaxf(__uint_as_float(s[0]), __uint_as_float(s[1]));
}
__device__ __forceinline__ void lds_barrier() { asm volatile("s_waitcnt lgkmcnt(0)\n\ts_barrier" ::: "memory"); }
__device__ __forceinline__ void lds_wait() { asm volatile("s_waitcnt lgkmcnt(0)" ::: "memory"); }
__device__ __forceinline__ void drain_vm() { asm volatile("s_waitcnt vmcnt(0)" ::: "memory"); }
__device__ __forceinline__ void word_store(unsigned char* ws, u32 off, u64 v) {
  __hip_atomic_store(reinterpret_cast<u64*>(ws + off), v, __ATOMIC_RELAXED, __HIP_MEMORY_SCOPE_AGENT);
}
__device__ __forceinline__ u64 word_load(unsigned char* ws, u32 off) {
  return __hip_atomic_load(reinterpret_cast<u64*>(ws + off), __ATOMIC_RELAXED, __HIP_MEMORY_SCOPE_AGENT);
}

#define STAMP(k) do {} while (0)

struct Ptrs {
  const u16* x; const u16* kA; const u16* vA; const u16* wqA; const u16* woA;
  const u16* kB; const u16* vB; const u16* wqB; const u16* woB; const u16* gw;
  u16* out; unsigned char* ws;
};

__global__ void __launch_bounds__(NTHR, 2) fused_decode(Ptrs P) {
  __shared__ __attribute__((aligned(16))) float lds[L_TOTAL];
  const int j = blockIdx.x, t = threadIdx.x, lane = t & 63;
  const int wave = __builtin_amdgcn_readfirstlane(t >> 6);
  unsigned char* ws = P.ws;
  STAMP(0);
  const u64 tag = *reinterpret_cast<const u64*>(ws + WS_EPOCH) + 1ull;
  const u64 tag48 = (tag & 0xFFFFFFFFFFFFull) << 16;
  const __amdgpu_buffer_rsrc_t rsW = __builtin_amdgcn_make_buffer_rsrc(ws, 0, (int)WS_BYTES, 0x00020000);

  int mod, h, s, k0, k1;
  if (j < 8 * NSA) { mod = 0; h = j / NSA; s = j - h * NSA; k0 = s * PERA; k1 = min(k0 + PERA, WINDOW); k0 += SK - WINDOW; k1 += SK - WINDOW; }
  else { const int jj = j - 8 * NSA; mod = 1; h = jj / NSB; s = jj - h * NSB; k0 = s * PERB; k1 = min(k0 + PERB, SK); }
#define INTERLEAVE 0
  const int nsp = mod ? NSB : NSA, kfirst = mod ? 0 : SK - WINDOW, npairs = mod ? SK / 2 : WINDOW / 2;
  const int np = INTERLEAVE ? npairs : (k1 - k0) >> 1;
#define PAIR_OF(i) (INTERLEAVE ? ((wave + 8 * (i)) * nsp + s) : (wave + 8 * (i)))
  const u16* kbase = (mod ? P.kB : P.kA) + (size_t)h * SK * DH;
  const u16* vbase = (mod ? P.vB : P.vA) + (size_t)h * SK * DH;
  const __amdgpu_buffer_rsrc_t rsK = __builtin_amdgcn_make_buffer_rsrc((void*)kbase, 0, SK * DH * 2, 0x00020000);
  const __amdgpu_buffer_rsrc_t rsV = __builtin_amdgcn_make_buffer_rsrc((void*)vbase, 0, SK * DH * 2, 0x00020000);
  const int voff = (lane >> 5) * 512 + (lane & 31) * 16;
#define KV_SOFF(i) (((INTERLEAVE ? kfirst : k0) + 2 * min(PAIR_OF(i), np - 1)) * 512)

  const bool qprod = !(j & 1);
  const int pj = j >> 1, mod1 = pj >> 6, col0 = (pj & 63) * 64;
  const u16* wq = mod1 ? P.wqB : P.wqA;
  const int seg = t & 7, r0 = t >> 3;
  u32x4 wv[7]; u16 xr[7];
  if (qprod) {
#pragma unroll
    for (int i = 0; i < 7; ++i) {
      const int row = r0 + 64 * i;
      wv[i] = __builtin_nontemporal_load(reinterpret_cast<const u32x4*>(wq + (size_t)row * 4096 + col0 + seg * 8));
      xr[i] = P.x[row];
    }
  }
  const int tg = min(t, DM - 1);
  const u16 gxr = P.x[tg], gwr = P.gw[tg];

  u32x4 Kr[NSLOT], Vr[NSLOT];
  if (wave >= 2) {
#pragma unroll
    for (int i = 0; i < EARLY; ++i) Kr[i] = __builtin_amdgcn_raw_buffer_load_b128(rsK, voff, KV_SOFF(i), AUX_NT);
  }
  const bool eodd = (EODD > 0) && (j & 1) && wave >= 2;
  if (eodd) {
#pragma unroll
    for (int i = EARLY; i < EARLY + EODD; ++i) Kr[i] = __builtin_amdgcn_raw_buffer_load_b128(rsK, voff, KV_SOFF(i), AUX_NT);
  }
  if (wave == 1) {
    const u32x4 z = {0u, 0u, 0u, 0u};
#pragma unroll
    for (int i = 0; i < 8; ++i) __builtin_amdgcn_raw_buffer_store_b128(z, rsW, (int)(WS_I1 + j * 8192 + (i * 64 + lane) * 16), 0, AUX_SC1);
    if (j < 112) {
#pragma unroll
      for (int i = 0; i < 8; ++i) __builtin_amdgcn_raw_buffer_store_b128(z, rsW, (int)(WS_I2 + j * 8192 + (i * 64 + lane) * 16), 0, AUX_SC1);
    }
  }
  __builtin_amdgcn_sched_barrier(0);

  const float gp = (t < DM) ? rbf(bf2f(gxr) * bf2f(gwr)) : 0.f;
  {
    if (qprod) {
      float acc[8];
#pragma unroll
      for (int k = 0; k < 8; ++k) acc[k] = 0.f;
#pragma unroll
      for (int i = 0; i < 7; ++i) {
        const float xv = bf2f(xr[i]);
#pragma unroll
        for (int k = 0; k < 4; ++k) { acc[2 * k] = fmaf(xv, bflo(wv[i][k]), acc[2 * k]); acc[2 * k + 1] = fmaf(xv, bfhi(wv[i][k]), acc[2 * k + 1]); }
      }
#pragma unroll
      for (int k = 0; k < 8; ++k) {
        float v = acc[k];
        v += dpp<XOR8>(v);
        v = swap16_add(v, v); v = swap32_add(v, v);
        acc[k] = v;
      }
      if (lane < 8) {
        *reinterpret_cast<f32x4*>(&lds[L_RED1 + wave * 64 + lane * 8]) = f32x4{acc[0], acc[1], acc[2], acc[3]};
        *reinterpret_cast<f32x4*>(&lds[L_RED1 + wave * 64 + lane * 8 + 4]) = f32x4{acc[4], acc[5], acc[6], acc[7]};
      }
    }
    float g = gp;
    g += dpp<XOR1>(g); g += dpp<XOR2>(g); g += dpp<ROR4>(g); g += dpp<XOR8>(g);
    g = swap16_add(g, g); g = swap32_add(g, g);
    if (lane == 0) lds[L_GP + wave] = g;
  }
  lds_barrier();
  STAMP(1);
  if (wave >= 2) {
#pragma unroll
    for (int i = EARLY; i < EARLY + MID; ++i) if (!(eodd && i < EARLY + EODD)) Kr[i] = __builtin_amdgcn_raw_buffer_load_b128(rsK, voff, KV_SOFF(i), AUX_NT);
#pragma unroll
    for (int i = 0; i < MIDV; ++i) Vr[i] = __builtin_amdgcn_raw_buffer_load_b128(rsV, voff, KV_SOFF(i), AUX_NT);
  }
  if (wave == 0) {
    if (qprod) {
      float a = lds[L_RED1 + lane];
#pragma unroll
      for (int w = 1; w < NWAVE; ++w) a += lds[L_RED1 + w * 64 + lane];
      word_store(ws, WS_QW + (mod1 * 4096 + col0 + lane) * 8, tag48 | (u64)f2bf_bits(a));
    }
    STAMP(2);
    u64 qw[8];
    for (unsigned spins = 0; spins < SPIN_CAP; ++spins) {
      bool ok = true;
#pragma unroll
      for (int k = 0; k < 8; ++k) { qw[k] = word_load(ws, WS_QW + (mod * 4096 + h * 512 + lane + 64 * k) * 8); }
      const u64 f0 = word_load(ws, WS_RDY + (mod * 128 + h * 16 + (lane & 15)) * 64);
      const u64 f1 = word_load(ws, WS_RDY + min(lane, 111) * 64);
      const u64 f2 = word_load(ws, WS_RDY + min(lane + 64, 111) * 64);
#pragma unroll
      for (int k = 0; k < 8; ++k) ok &= (qw[k] & ~0xFFFFull) == tag48;
      ok &= (f0 == tag) && (f1 == tag) && (f2 == tag);
      if (__all(ok)) break;
      __builtin_amdgcn_s_sleep(2);
    }
    u16* ql = reinterpret_cast<u16*>(&lds[L_Q]);
#pragma unroll
    for (int k = 0; k < 8; ++k) ql[lane + 64 * k] = (u16)qw[k];
    STAMP(3);
  } else if (wave == 1) {
    drain_vm();
    if (lane == 0) word_store(ws, WS_RDY + j * 64, tag);
  } else if (wave == 2 && lane == 0) {
    float g = 0.f;
#pragma unroll
    for (int w = 0; w < NWAVE; ++w) g += lds[L_GP + w];
    g = rbf(g);
    lds[L_GP + 8] = rbf(1.f / (1.f + __expf(-g)));
  }
  lds_barrier();

  if (wave < 2) {
#pragma unroll
    for (int i = 0; i < EARLY + MID; ++i) Kr[i] = __builtin_amdgcn_raw_buffer_load_b128(rsK, voff, KV_SOFF(i), AUX_NT);
  }
#pragma unroll
  for (int i = EARLY + MID; i < NSLOT; ++i) Kr[i] = __builtin_amdgcn_raw_buffer_load_b128(rsK, voff, KV_SOFF(i), AUX_NT);
  if (wave < 2) {
#pragma unroll
    for (int i = 0; i < MIDV; ++i) Vr[i] = __builtin_amdgcn_raw_buffer_load_b128(rsV, voff, KV_SOFF(i), AUX_NT);
  }
#pragma unroll
  for (int i = MIDV; i < NSLOT; ++i) Vr[i] = __builtin_amdgcn_raw_buffer_load_b128(rsV, voff, KV_SOFF(i), AUX_NT);
  __builtin_amdgcn_sched_barrier(0);
  STAMP(4);

  const int c = lane & 31, half = lane >> 5;
  u32x4 qk[2];
  qk[0] = *reinterpret_cast<const u32x4*>(&lds[L_Q + c * 4]);
  qk[1] = *reinterpret_cast<const u32x4*>(&lds[L_Q + 128 + c * 4]);
  const bool b3 = lane & 8, b2 = lane & 4, b1 = lane & 2;
  const int ulane = ((lane >> 4) & 1) * 4 + ((lane >> 3) & 1) * 2 + ((lane >> 2) & 1);
  constexpr float C2 = 0.0625f * 1.4426950408889634f;
  float tsc[3];
#pragma unroll
  for (int gi = 0; gi < 3; ++gi) {
    float xx[16];
#pragma unroll
    for (int u = 0; u < 8; ++u) {
      const int i = gi * 8 + u;
#pragma unroll
      for (int g = 0; g < 2; ++g) {
        float d = 0.f;
        if (i < NSLOT) {
#pragma unroll
          for (int k = 0; k < 4; ++k) d = dot2(Kr[i][k], qk[g][k], d);
        }
        xx[u * 2 + g] = d;
      }
    }
    float y[8], z[4], w2[2];
#pragma unroll
    for (int v = 0; v < 8; ++v) y[v] = swap16_add(xx[v], xx[v + 8]);
#pragma unroll
    for (int v = 0; v < 4; ++v) { const float keep = b3 ? y[v + 4] : y[v], send = b3 ? y[v] : y[v + 4]; z[v] = keep + dpp<XOR8>(send); }
#pragma unroll
    for (int v = 0; v < 2; ++v) { const float keep = b2 ? z[v + 2] : z[v], send = b2 ? z[v] : z[v + 2]; w2[v] = keep + dpp<XOR7>(send); }
    float sfin = (b1 ? w2[1] : w2[0]) + dpp<XOR2>(b1 ? w2[0] : w2[1]);
    sfin += dpp<XOR1>(sfin);
    const bool valid = (INTERLEAVE ? ((wave + 8 * (gi * 8 + ulane)) * nsp + s) : (wave + 8 * (gi * 8 + ulane))) < np;
    tsc[gi] = valid ? rbf(sfin) * C2 : -INFINITY;
  }
  float mx = fmaxf(fmaxf(tsc[0], tsc[1]), tsc[2]);
  mx = fmaxf(mx, dpp<ROR4>(mx)); mx = fmaxf(mx, dpp<XOR8>(mx)); mx = x16_max(mx); mx = x32_max(mx);
  float pv[3], ls = 0.f;
#pragma unroll
  for (int gi = 0; gi < 3; ++gi) { pv[gi] = __builtin_amdgcn_exp2f(tsc[gi] - mx); ls += pv[gi]; }
  ls += dpp<ROR4>(ls); ls += dpp<XOR8>(ls); ls = swap16_add(ls, ls); ls = swap32_add(ls, ls);
  {
    float* pb = &lds[L_P + (wave * 2 + half) * 48];
    if (!(lane & 1)) {
#pragma unroll
      for (int gi = 0; gi < 3; ++gi) pb[(gi * 8 + ulane) * 2 + (b1 ? 1 : 0)] = pv[gi];
    }
    if (lane == 0 || lane == 2) { lds[L_ML + wave * 4 + (b1 ? 2 : 0)] = mx; lds[L_ML + wave * 4 + (b1 ? 2 : 0) + 1] = ls; }
  }
  lds_wait();
  STAMP(5);
  float acc[2][8];
#pragma unroll
  for (int g = 0; g < 2; ++g)
#pragma unroll
    for (int k = 0; k < 8; ++k) acc[g][k] = 0.f;
  {
    const float* pb = &lds[L_P + (wave * 2 + half) * 48];
#pragma unroll
    for (int i2 = 0; i2 < (NSLOT + 1) / 2; ++i2) {
      const f32x4 pp = *reinterpret_cast<const f32x4*>(pb + i2 * 4);
#pragma unroll
      for (int e = 0; e < 2; ++e) {
        const int i = 2 * i2 + e;
        if (i < NSLOT) {
          float vf[8];
#pragma unroll
          for (int k = 0; k < 4; ++k) { vf[2 * k] = bflo(Vr[i][k]); vf[2 * k + 1] = bfhi(Vr[i][k]); }
#pragma unroll
          for (int g = 0; g < 2; ++g)
#pragma unroll
            for (int k = 0; k < 8; ++k) acc[g][k] = fmaf(pp[e * 2 + g], vf[k], acc[g][k]);
        }
      }
    }
  }
  {
    float o8[8];
#pragma unroll
    for (int k = 0; k < 8; ++k) o8[k] = swap32_add(acc[0][k], acc[1][k]);
    float* mg = &lds[L_MRG + wave * 512 + half * 256 + c * 8];
    *reinterpret_cast<f32x4*>(mg) = f32x4{o8[0], o8[1], o8[2], o8[3]};
    *reinterpret_cast<f32x4*>(mg + 4) = f32x4{o8[4], o8[5], o8[6], o8[7]};
  }
  lds_barrier();
  STAMP(6);
  {
    const int g = t >> 8, d = t & 255;
    float M = -INFINITY;
#pragma unroll
    for (int w = 0; w < NWAVE; ++w) M = fmaxf(M, lds[L_ML + w * 4 + g * 2]);
    float L = 0.f, o = 0.f;
#pragma unroll
    for (int w = 0; w < NWAVE; ++w) {
      const float wt = __builtin_amdgcn_exp2f(lds[L_ML + w * 4 + g * 2] - M);
      L = fmaf(wt, lds[L_ML + w * 4 + g * 2 + 1], L);
      o = fmaf(wt, lds[L_MRG + w * 512 + g * 256 + d], o);
    }
    const u32 ib = WS_I1 + (mod * 128 + (2 * h + g) * 8 + (d >> 5)) * 8192 + s * 34 * 8;
    word_store(ws, ib + (d & 31) * 8, FULL | (u64)__float_as_uint(o));
    if ((d & 31) == 0) { word_store(ws, ib + 32 * 8, FULL | (u64)__float_as_uint(M)); word_store(ws, ib + 33 * 8, FULL | (u64)__float_as_uint(L)); }
  }
  STAMP(7);

  const int mod3 = j >> 7, hq3 = (j & 127) >> 3, row3 = (j & 127) * 32;
  const int rg3 = t / 56, cg3 = t - rg3 * 56;
  u32x4 Wo[4];
  {
    const u16* wo = (mod3 ? P.woB : P.woA) + (size_t)row3 * DM;
    const int tr = min(t, 447), rgc = tr / 56, cgc = tr - rgc * 56;
#pragma unroll
    for (int i = 0; i < 4; ++i) Wo[i] = __builtin_nontemporal_load(reinterpret_cast<const u32x4*>(wo + (size_t)(rgc + 8 * i) * DM + cgc * 8));
  }
  const int ns3 = mod3 ? NSB : NSA;
  {
    const int dd = lane & 31, hs = lane >> 5;
    const int sA = wave + 8 * hs, sB = wave + 16 + 8 * hs;
    const bool nA = sA < ns3, nB = sB < ns3, nM = lane < ns3;
    const u32 ib = WS_I1 + j * 8192;
    u64 wA = 0ull, wB = 0ull, wM = 0ull, wL = 0ull;
    for (unsigned spins = 0; spins < SPIN_CAP; ++spins) {
      if (nA && !(wA & FULL)) wA = word_load(ws, ib + (sA * 34 + dd) * 8);
      if (nB && !(wB & FULL)) wB = word_load(ws, ib + (sB * 34 + dd) * 8);
      if (nM && !(wM & FULL)) wM = word_load(ws, ib + (lane * 34 + 32) * 8);
      if (nM && !(wL & FULL)) wL = word_load(ws, ib + (lane * 34 + 33) * 8);
      const bool ok = (!nA || (wA & FULL)) && (!nB || (wB & FULL)) && (!nM || ((wM & wL) & FULL));
      if (__all(ok)) break;
      __builtin_amdgcn_s_sleep(1);
    }
    STAMP(8);
    const float ms = nM ? __uint_as_float((u32)wM) : -INFINITY;
    const float lsv = nM ? __uint_as_float((u32)wL) : 0.f;
    float M = ms;
    M = fmaxf(M, dpp<XOR1>(M)); M = fmaxf(M, dpp<XOR2>(M)); M = fmaxf(M, dpp<ROR4>(M)); M = fmaxf(M, dpp<XOR8>(M)); M = x16_max(M); M = x32_max(M);
    const float wt = nM ? __builtin_amdgcn_exp2f(ms - M) : 0.f;
    float L = wt * lsv;
    L += dpp<XOR1>(L); L += dpp<XOR2>(L); L += dpp<ROR4>(L); L += dpp<XOR8>(L); L = swap16_add(L, L); L = swap32_add(L, L);
    const float cw = wt / L;
    const float cA = __shfl(cw, sA, 64), cB = __shfl(cw, sB, 64);
    float v = (nA ? cA * __uint_as_float((u32)wA) : 0.f) + (nB ? cB * __uint_as_float((u32)wB) : 0.f);
    v = swap32_add(v, v);
    if (lane < 32) lds[L_RED2 + wave * 32 + dd] = v;
  }
  lds_barrier();
  STAMP(9);
  if (t < 448) {
    float pacc[8];
#pragma unroll
    for (int k = 0; k < 8; ++k) pacc[k] = 0.f;
#pragma unroll
    for (int i = 0; i < 4; ++i) {
      const int row = rg3 + 8 * i;
      float ov = 0.f;
#pragma unroll
      for (int w = 0; w < NWAVE; ++w) ov += lds[L_RED2 + w * 32 + row];
      ov = rbf(ov);
#pragma unroll
      for (int k = 0; k < 4; ++k) { pacc[2 * k] = fmaf(ov, bflo(Wo[i][k]), pacc[2 * k]); pacc[2 * k + 1] = fmaf(ov, bfhi(Wo[i][k]), pacc[2 * k + 1]); }
    }
    float* r3 = &lds[L_RED3 + rg3 * 448 + cg3 * 8];
    *reinterpret_cast<f32x4*>(r3) = f32x4{pacc[0], pacc[1], pacc[2], pacc[3]};
    *reinterpret_cast<f32x4*>(r3 + 4) = f32x4{pacc[4], pacc[5], pacc[6], pacc[7]};
  }
  lds_barrier();
  STAMP(10);
  if (t < 448) {
    float sacc = lds[L_RED3 + t];
#pragma unroll
    for (int rg = 1; rg < 8; ++rg) sacc += lds[L_RED3 + rg * 448 + t];
    word_store(ws, WS_I2 + (t >> 2) * 8192 + (j * 4 + (t & 3)) * 8, FULL | (u64)__float_as_uint(sacc));
  }
  STAMP(11);

  if (j < 112) {
    f32x4 v = {0.f, 0.f, 0.f, 0.f};
    if (t < 256) {
      u64 w[4] = {0ull, 0ull, 0ull, 0ull};
      for (unsigned spins = 0; spins < SPIN_CAP; ++spins) {
#pragma unroll
        for (int k = 0; k < 4; ++k) if (!(w[k] & FULL)) w[k] = word_load(ws, WS_I2 + j * 8192 + (t * 4 + k) * 8);
        const bool ok = (w[0] & w[1] & w[2] & w[3] & FULL) != 0ull;
        if (__all(ok)) break;
        __builtin_amdgcn_s_sleep(2);
      }
#pragma unroll
      for (int k = 0; k < 4; ++k) {
        float q = __uint_as_float((u32)w[k]);
        q += dpp<XOR1>(q); q += dpp<XOR2>(q); q += dpp<ROR4>(q); q += dpp<XOR8>(q); q = swap16_add(q, q); q = swap32_add(q, q);
        v[k] = q;
      }
      if (lane == 0) *reinterpret_cast<f32x4*>(&lds[L_FIN + wave * 4]) = v;
    }
    lds_barrier();
    STAMP(12);
    if (t == 0) {
      const float g = lds[L_GP + 8], og = rbf(1.f - g);
      u32 o[4];
#pragma unroll
      for (int k = 0; k < 4; ++k) {
        const float a = rbf(lds[L_FIN + k] + lds[L_FIN + 4 + k]), b = rbf(lds[L_FIN + 8 + k] + lds[L_FIN + 12 + k]);
        o[k] = f2bf_bits(rbf(g * a) + rbf(og * b));
      }
      *reinterpret_cast<u32x2*>(P.out + j * 4) = u32x2{o[0] | (o[1] << 16), o[2] | (o[3] << 16)};
      if (j == 0) *reinterpret_cast<u64*>(ws + WS_EPOCH) = tag;
    }
  }
  STAMP(13);
}
}

extern "C" void kernel_launch(void* const* d_in, const int* in_sizes, int n_in, void* d_out, int out_size, void* d_ws, size_t ws_size,
                              hipStream_t stream) {
  Ptrs P;
  P.x = (const u16*)d_in[0]; P.kA = (const u16*)d_in[1]; P.vA = (const u16*)d_in[2]; P.wqA = (const u16*)d_in[3]; P.woA = (const u16*)d_in[4];
  P.kB = (const u16*)d_in[6]; P.vB = (const u16*)d_in[7]; P.wqB = (const u16*)d_in[8]; P.woB = (const u16*)d_in[9]; P.gw = (const u16*)d_in[10];
  P.out = (u16*)d_out; P.ws = (unsigned char*)d_ws;
  hipLaunchKernelGGL(fused_decode, dim3(256), dim3(NTHR), 0, stream, P);
}
```

```cpp
#include <hip/hip_runtime.h>
#include <hip/hip_bf16.h>
#include <stdint.h>

namespace {
typedef unsigned short u16;
typedef unsigned int u32;
typedef unsigned long long u64;
typedef __attribute__((ext_vector_type(4))) unsigned u32x4;
typedef __attribute__((ext_vector_type(2))) unsigned u32x2;
typedef __attribute__((ext_vector_type(4))) float f32x4;
typedef __attribute__((ext_vector_type(2))) __bf16 bf16x2;

constexpr int DM = 448, SK = 8192, DH = 256, WINDOW = 2048;
constexpr int NSA = 7, NSB = 25;
constexpr int PERA = 294, PERB = 328;
constexpr int NTHR = 512, NWAVE = 8;
constexpr int NSLOT = 21;
#define EARLY_K 0
constexpr int EARLY = EARLY_K;
#define MID_K 14
constexpr int MID = MID_K;
#define BARRIER_1B 1
#define MID_V 0
constexpr int MIDV = MID_V;

constexpr u32 WS_EPOCH = 0;
constexpr u32 WS_RDY = 0x1000;
constexpr u32 WS_QW = 0x10000;
constexpr u32 WS_I1 = 0x100000;
constexpr u32 WS_I2 = 0x300000;
constexpr u32 WS_BYTES = 0x400000;
constexpr int AUX_SC1 = 16;
#define KV_AUX 2
constexpr int AUX_NT = KV_AUX;
constexpr unsigned SPIN_CAP = 1u << 17;
#define POLL_SLEEP 1
constexpr u64 FULL = 1ull << 32;

constexpr int L_RED1 = 0;
constexpr int L_GP = 512;
constexpr int L_Q = 544;
constexpr int L_P = 800;
constexpr int L_ML = 1568;
constexpr int L_MRG = 1600;
constexpr int L_ST1 = L_MRG + 4096;
constexpr int L_RED2 = L_ST1 + 1024;
constexpr int L_RED3 = L_RED2 + 256;
constexpr int L_FIN = L_RED3 + 3584;
constexpr int L_TOTAL = L_FIN + 16;

__device__ __forceinline__ float bf2f(u16 h) { return __uint_as_float(((u32)h) << 16); }
__device__ __forceinline__ float bflo(u32 w) { return __uint_as_float(w << 16); }
__device__ __forceinline__ float bfhi(u32 w) { return __uint_as_float(w & 0xffff0000u); }
__device__ __forceinline__ u32 f2bf_bits(float f) { return (u32)__builtin_bit_cast(u16, (__bf16)f); }
__device__ __forceinline__ float rbf(float f) { return __uint_as_float(f2bf_bits(f) << 16); }
__device__ __forceinline__ u32 pack2(float lo, float hi) { return f2bf_bits(lo) | (f2bf_bits(hi) << 16); }
__device__ __forceinline__ float dot2(u32 a, u32 b, float c) {
  return __builtin_amdgcn_fdot2_f32_bf16(__builtin_bit_cast(bf16x2, a), __builtin_bit_cast(bf16x2, b), c, false);
}
template <int CTRL> __device__ __forceinline__ float dpp(float x) {
  return __builtin_bit_cast(float, __builtin_amdgcn_mov_dpp(__builtin_bit_cast(int, x), CTRL, 0xf, 0xf, true));
}
constexpr int XOR1 = 0xB1, XOR2 = 0x4E, XOR7 = 0x141, XOR8 = 0x128, ROR4 = 0x124;
__device__ __forceinline__ float swap16_add(float a, float b) {
  auto s = __builtin_amdgcn_permlane16_swap(__float_as_uint(a), __float_as_uint(b), false, false);
  return __uint_as_float(s[0]) + __uint_as_float(s[1]);
}
__device__ __forceinline__ float swap32_add(float a, float b) {
  auto s = __builtin_amdgcn_permlane32_swap(__float_as_uint(a), __float_as_uint(b), false, false);
  return __uint_as_float(s[0]) + __uint_as_float(s[1]);
}
__device__ __forceinline__ float x16_max(float x) {
  auto s = __builtin_amdgcn_permlane16_swap(__float_as_uint(x), __float_as_uint(x), false, false);
  return fmaxf(__uint_as_float(s[0]), __uint_as_float(s[1]));
}
__device__ __forceinline__ float x32_max(float x) {
  auto s = __builtin_amdgcn_permlane32_swap(__float_as_uint(x), __float_as_uint(x), false, false);
  return fmaxf(__uint_as_float(s[0]), __uint_as_float(s[1]));
}
__device__ __forceinline__ void lds_barrier() { asm volatile("s_waitcnt lgkmcnt(0)\n\ts_barrier" ::: "memory"); }
__device__ __forceinline__ void lds_wait() { asm volatile("s_waitcnt lgkmcnt(0)" ::: "memory"); }
__device__ __forceinline__ void drain_vm() { asm volatile("s_waitcnt vmcnt(0)" ::: "memory"); }
__device__ __forceinline__ void word_store(unsigned char* ws, u32 off, u64 v) {
  __hip_atomic_store(reinterpret_cast<u64*>(ws + off), v, __ATOMIC_RELAXED, __HIP_MEMORY_SCOPE_AGENT);
}
__device__ __forceinline__ u64 word_load(unsigned char* ws, u32 off) {
  return __hip_atomic_load(reinterpret_cast<u64*>(ws + off), __ATOMIC_RELAXED, __HIP_MEMORY_SCOPE_AGENT);
}

#define STAMP(k) do {} while (0)

struct Ptrs {
  const u16* x; const u16* kA; const u16* vA; const u16* wqA; const u16* woA;
  const u16* kB; const u16* vB; const u16* wqB; const u16* woB; const u16* gw;
  u16* out; unsigned char* ws;
};

__global__ void __launch_bounds__(NTHR, 2) fused_decode(Ptrs P) {
  __shared__ __attribute__((aligned(16))) float lds[L_TOTAL];
  const int j = blockIdx.x, t = threadIdx.x, lane = t & 63;
  const int wave = __builtin_amdgcn_readfirstlane(t >> 6);
  unsigned char* ws = P.ws;
  STAMP(0);
  const u64 tag = *reinterpret_cast<const u64*>(ws + WS_EPOCH) + 1ull;
  const u64 tag48 = (tag & 0xFFFFFFFFFFFFull) << 16;
  const __amdgpu_buffer_rsrc_t rsW = __builtin_amdgcn_make_buffer_rsrc(ws, 0, (int)WS_BYTES, 0x00020000);

  int mod, h, s, k0, k1;
  if (j < 8 * NSA) { mod = 0; h = j / NSA; s = j - h * NSA; k0 = s * PERA; k1 = min(k0 + PERA, WINDOW); k0 += SK - WINDOW; k1 += SK - WINDOW; }
  else { const int jj = j - 8 * NSA; mod = 1; h = jj / NSB; s = jj - h * NSB; k0 = s * PERB; k1 = min(k0 + PERB, SK); }
#define INTERLEAVE 0
  const int nsp = mod ? NSB : NSA, kfirst = mod ? 0 : SK - WINDOW, npairs = mod ? SK / 2 : WINDOW / 2;
  const int np = INTERLEAVE ? npairs : (k1 - k0) >> 1;
#define PAIR_OF(i) (INTERLEAVE ? ((wave + 8 * (i)) * nsp + s) : (wave + 8 * (i)))
  const u16* kbase = (mod ? P.kB : P.kA) + (size_t)h * SK * DH;
  const u16* vbase = (mod ? P.vB : P.vA) + (size_t)h * SK * DH;
  const __amdgpu_buffer_rsrc_t rsK = __builtin_amdgcn_make_buffer_rsrc((void*)kbase, 0, SK * DH * 2, 0x00020000);
  const __amdgpu_buffer_rsrc_t rsV = __builtin_amdgcn_make_buffer_rsrc((void*)vbase, 0, SK * DH * 2, 0x00020000);
  const int voff = (lane >> 5) * 512 + (lane & 31) * 16;
#define KV_SOFF(i) (((INTERLEAVE ? kfirst : k0) + 2 * min(PAIR_OF(i), np - 1)) * 512)

  const bool qprod = !(j & 1);
  const int pj = j >> 1, mod1 = pj >> 6, col0 = (pj & 63) * 64;
  const u16* wq = mod1 ? P.wqB : P.wqA;
  const int seg = t & 7, r0 = t >> 3;
  u32x4 wv[7]; u16 xr[7];
  if (qprod) {
#pragma unroll
    for (int i = 0; i < 7; ++i) {
      const int row = r0 + 64 * i;
      wv[i] = __builtin_nontemporal_load(reinterpret_cast<const u32x4*>(wq + (size_t)row * 4096 + col0 + seg * 8));
      xr[i] = P.x[row];
    }
  }
  const int tg = min(t, DM - 1);
  const u16 gxr = P.x[tg], gwr = P.gw[tg];

  u32x4 Kr[NSLOT], Vr[NSLOT];
  if (wave >= 2) {
#pragma unroll
    for (int i = 0; i < EARLY; ++i) Kr[i] = __builtin_amdgcn_raw_buffer_load_b128(rsK, voff, KV_SOFF(i), AUX_NT);
  }
  if (wave == 1) {
    const u32x4 z = {0u, 0u, 0u, 0u};
#pragma unroll
    for (int i = 0; i < 8; ++i) __builtin_amdgcn_raw_buffer_store_b128(z, rsW, (int)(WS_I1 + j * 8192 + (i * 64 + lane) * 16), 0, AUX_SC1);
    if (j < 112) {
#pragma unroll
      for (int i = 0; i < 8; ++i) __builtin_amdgcn_raw_buffer_store_b128(z, rsW, (int)(WS_I2 + j * 8192 + (i * 64 + lane) * 16), 0, AUX_SC1);
    }
  }
  __builtin_amdgcn_sched_barrier(0);

  const float gp = (t < DM) ? rbf(bf2f(gxr) * bf2f(gwr)) : 0.f;
  {
    if (qprod) {
      float acc[8];
#pragma unroll
      for (int k = 0; k < 8; ++k) acc[k] = 0.f;
#pragma unroll
      for (int i = 0; i < 7; ++i) {
        const float xv = bf2f(xr[i]);
#pragma unroll
        for (int k = 0; k < 4; ++k) { acc[2 * k] = fmaf(xv, bflo(wv[i][k]), acc[2 * k]); acc[2 * k + 1] = fmaf(xv, bfhi(wv[i][k]), acc[2 * k + 1]); }
      }
#pragma unroll
      for (int k = 0; k < 8; ++k) {
        float v = acc[k];
        v += dpp<XOR8>(v);
        v = swap16_add(v, v); v = swap32_add(v, v);
        acc[k] = v;
      }
      if (lane < 8) {
        *reinterpret_cast<f32x4*>(&lds[L_RED1 + wave * 64 + lane * 8]) = f32x4{acc[0], acc[1], acc[2], acc[3]};
        *reinterpret_cast<f32x4*>(&lds[L_RED1 + wave * 64 + lane * 8 + 4]) = f32x4{acc[4], acc[5], acc[6], acc[7]};
      }
    }
    float g = gp;
    g += dpp<XOR1>(g); g += dpp<XOR2>(g); g += dpp<ROR4>(g); g += dpp<XOR8>(g);
    g = swap16_add(g, g); g = swap32_add(g, g);
    if (lane == 0) lds[L_GP + wave] = g;
  }
  lds_barrier();
  STAMP(1);
  if (wave == 0) {
    if (qprod) {
      float a = lds[L_RED1 + lane];
#pragma unroll
      for (int w = 1; w < NWAVE; ++w) a += lds[L_RED1 + w * 64 + lane];
      word_store(ws, WS_QW + (mod1 * 4096 + col0 + lane) * 8, tag48 | (u64)f2bf_bits(a));
    }
  } else if (wave == 1) {
    drain_vm();
    if (lane == 0) word_store(ws, WS_RDY + j * 64, tag);
  }
  __builtin_amdgcn_s_barrier();
  if (wave >= 2) {
#pragma unroll
    for (int i = EARLY; i < EARLY + MID; ++i) Kr[i] = __builtin_amdgcn_raw_buffer_load_b128(rsK, voff, KV_SOFF(i), AUX_NT);
#pragma unroll
    for (int i = 0; i < MIDV; ++i) Vr[i] = __builtin_amdgcn_raw_buffer_load_b128(rsV, voff, KV_SOFF(i), AUX_NT);
  }
  if (wave == 0) {
    STAMP(2);
    u64 qw[8];
    for (unsigned spins = 0; spins < SPIN_CAP; ++spins) {
      bool ok = true;
#pragma unroll
      for (int k = 0; k < 8; ++k) { qw[k] = word_load(ws, WS_QW + (mod * 4096 + h * 512 + lane + 64 * k) * 8); }
      const u64 f0 = word_load(ws, WS_RDY + (mod * 128 + h * 16 + (lane & 15)) * 64);
      const u64 f1 = word_load(ws, WS_RDY + min(lane, 111) * 64);
      const u64 f2 = word_load(ws, WS_RDY + min(lane + 64, 111) * 64);
#pragma unroll
      for (int k = 0; k < 8; ++k) ok &= (qw[k] & ~0xFFFFull) == tag48;
      ok &= (f0 == tag) && (f1 == tag) && (f2 == tag);
      if (__all(ok)) break;
      __builtin_amdgcn_s_sleep(POLL_SLEEP);
    }
    u16* ql = reinterpret_cast<u16*>(&lds[L_Q]);
#pragma unroll
    for (int k = 0; k < 8; ++k) ql[lane + 64 * k] = (u16)qw[k];
    STAMP(3);
  } else if (wave == 2 && lane == 0) {
    float g = 0.f;
#pragma unroll
    for (int w = 0; w < NWAVE; ++w) g += lds[L_GP + w];
    g = rbf(g);
    lds[L_GP + 8] = rbf(1.f / (1.f + __expf(-g)));
  }
  lds_barrier();

  if (wave < 2) {
#pragma unroll
    for (int i = 0; i < EARLY + MID; ++i) Kr[i] = __builtin_amdgcn_raw_buffer_load_b128(rsK, voff, KV_SOFF(i), AUX_NT);
  }
#pragma unroll
  for (int i = EARLY + MID; i < NSLOT; ++i) Kr[i] = __builtin_amdgcn_raw_buffer_load_b128(rsK, voff, KV_SOFF(i), AUX_NT);
  if (wave < 2) {
#pragma unroll
    for (int i = 0; i < MIDV; ++i) Vr[i] = __builtin_amdgcn_raw_buffer_load_b128(rsV, voff, KV_SOFF(i), AUX_NT);
  }
#pragma unroll
  for (int i = MIDV; i < NSLOT; ++i) Vr[i] = __builtin_amdgcn_raw_buffer_load_b128(rsV, voff, KV_SOFF(i), AUX_NT);
  __builtin_amdgcn_sched_barrier(0);
  STAMP(4);

  const int c = lane & 31, half = lane >> 5;
  u32x4 qk[2];
  qk[0] = *reinterpret_cast<const u32x4*>(&lds[L_Q + c * 4]);
  qk[1] = *reinterpret_cast<const u32x4*>(&lds[L_Q + 128 + c * 4]);
  const bool b3 = lane & 8, b2 = lane & 4, b1 = lane & 2;
  const int ulane = ((lane >> 4) & 1) * 4 + ((lane >> 3) & 1) * 2 + ((lane >> 2) & 1);
  constexpr float C2 = 0.0625f * 1.4426950408889634f;
  float tsc[3];
#pragma unroll
  for (int gi = 0; gi < 3; ++gi) {
    float xx[16];
#pragma unroll
    for (int u = 0; u < 8; ++u) {
      const int i = gi * 8 + u;
#pragma unroll
      for (int g = 0; g < 2; ++g) {
        float d = 0.f;
        if (i < NSLOT) {
#pragma unroll
          for (int k = 0; k < 4; ++k) d = dot2(Kr[i][k], qk[g][k], d);
        }
        xx[u * 2 + g] = d;
      }
    }
    float y[8], z[4], w2[2];
#pragma unroll
    for (int v = 0; v < 8; ++v) y[v] = swap16_add(xx[v], xx[v + 8]);
#pragma unroll
    for (int v = 0; v < 4; ++v) { const float keep = b3 ? y[v + 4] : y[v], send = b3 ? y[v] : y[v + 4]; z[v] = keep + dpp<XOR8>(send); }
#pragma unroll
    for (int v = 0; v < 2; ++v) { const float keep = b2 ? z[v + 2] : z[v], send = b2 ? z[v] : z[v + 2]; w2[v] = keep + dpp<XOR7>(send); }
    float sfin = (b1 ? w2[1] : w2[0]) + dpp<XOR2>(b1 ? w2[0] : w2[1]);
    sfin += dpp<XOR1>(sfin);
    const bool valid = (INTERLEAVE ? ((wave + 8 * (gi * 8 + ulane)) * nsp + s) : (wave + 8 * (gi * 8 + ulane))) < np;
    tsc[gi] = valid ? rbf(sfin) * C2 : -INFINITY;
  }
  float mx = fmaxf(fmaxf(tsc[0], tsc[1]), tsc[2]);
  mx = fmaxf(mx, dpp<ROR4>(mx)); mx = fmaxf(mx, dpp<XOR8>(mx)); mx = x16_max(mx); mx = x32_max(mx);
  float pv[3], ls = 0.f;
#pragma unroll
  for (int gi = 0; gi < 3; ++gi) { pv[gi] = __builtin_amdgcn_exp2f(tsc[gi] - mx); ls += pv[gi]; }
  ls += dpp<ROR4>(ls); ls += dpp<XOR8>(ls); ls = swap16_add(ls, ls); ls = swap32_add(ls, ls);
  {
    float* pb = &lds[L_P + (wave * 2 + half) * 48];
    if (!(lane & 1)) {
#pragma unroll
      for (int gi = 0; gi < 3; ++gi) pb[(gi * 8 + ulane) * 2 + (b1 ? 1 : 0)] = pv[gi];
    }
    if (lane == 0 || lane == 2) { lds[L_ML + wave * 4 + (b1 ? 2 : 0)] = mx; lds[L_ML + wave * 4 + (b1 ? 2 : 0) + 1] = ls; }
  }
  lds_wait();
  STAMP(5);
  float acc[2][8];
#pragma unroll
  for (int g = 0; g < 2; ++g)
#pragma unroll
    for (int k = 0; k < 8; ++k) acc[g][k] = 0.f;
  {
    const float* pb = &lds[L_P + (wave * 2 + half) * 48];
#pragma unroll
    for (int i2 = 0; i2 < (NSLOT + 1) / 2; ++i2) {
      const f32x4 pp = *reinterpret_cast<const f32x4*>(pb + i2 * 4);
#pragma unroll
      for (int e = 0; e < 2; ++e) {
        const int i = 2 * i2 + e;
        if (i < NSLOT) {
          float vf[8];
#pragma unroll
          for (int k = 0; k < 4; ++k) { vf[2 * k] = bflo(Vr[i][k]); vf[2 * k + 1] = bfhi(Vr[i][k]); }
#pragma unroll
          for (int g = 0; g < 2; ++g)
#pragma unroll
            for (int k = 0; k < 8; ++k) acc[g][k] = fmaf(pp[e * 2 + g], vf[k], acc[g][k]);
        }
      }
    }
  }
  {
    float o8[8];
#pragma unroll
    for (int k = 0; k < 8; ++k) o8[k] = swap32_add(acc[0][k], acc[1][k]);
    float* mg = &lds[L_MRG + wave * 512 + half * 256 + c * 8];
    *reinterpret_cast<f32x4*>(mg) = f32x4{o8[0], o8[1], o8[2], o8[3]};
    *reinterpret_cast<f32x4*>(mg + 4) = f32x4{o8[4], o8[5], o8[6], o8[7]};
  }
  lds_barrier();
  STAMP(6);
  {
    const int g = t >> 8, d = t & 255;
    float M = -INFINITY;
#pragma unroll
    for (int w = 0; w < NWAVE; ++w) M = fmaxf(M, lds[L_ML + w * 4 + g * 2]);
    float L = 0.f, o = 0.f;
#pragma unroll
    for (int w = 0; w < NWAVE; ++w) {
      const float wt = __builtin_amdgcn_exp2f(lds[L_ML + w * 4 + g * 2] - M);
      L = fmaf(wt, lds[L_ML + w * 4 + g * 2 + 1], L);
      o = fmaf(wt, lds[L_MRG + w * 512 + g * 256 + d], o);
    }
    const u32 ib = WS_I1 + (mod * 128 + (2 * h + g) * 8 + (d >> 5)) * 8192 + s * 34 * 8;
    word_store(ws, ib + (d & 31) * 8, FULL | (u64)__float_as_uint(o));
    if ((d & 31) == 0) { word_store(ws, ib + 32 * 8, FULL | (u64)__float_as_uint(M)); word_store(ws, ib + 33 * 8, FULL | (u64)__float_as_uint(L)); }
  }
  STAMP(7);

  const int mod3 = j >> 7, hq3 = (j & 127) >> 3, row3 = (j & 127) * 32;
  const int rg3 = t / 56, cg3 = t - rg3 * 56;
  u32x4 Wo[4];
  {
    const u16* wo = (mod3 ? P.woB : P.woA) + (size_t)row3 * DM;
    const int tr = min(t, 447), rgc = tr / 56, cgc = tr - rgc * 56;
#pragma unroll
    for (int i = 0; i < 4; ++i) Wo[i] = __builtin_nontemporal_load(reinterpret_cast<const u32x4*>(wo + (size_t)(rgc + 8 * i) * DM + cgc * 8));
  }
  const int ns3 = mod3 ? NSB : NSA;
  {
    const int dd = lane & 31, hs = lane >> 5;
    const int sA = wave + 8 * hs, sB = wave + 16 + 8 * hs;
    const bool nA = sA < ns3, nB = sB < ns3, nM = lane < ns3;
    const u32 ib = WS_I1 + j * 8192;
    u64 wA = 0ull, wB = 0ull, wM = 0ull, wL = 0ull;
    for (unsigned spins = 0; spins < SPIN_CAP; ++spins) {
      if (nA && !(wA & FULL)) wA = word_load(ws, ib + (sA * 34 + dd) * 8);
      if (nB && !(wB & FULL)) wB = word_load(ws, ib + (sB * 34 + dd) * 8);
      if (nM && !(wM & FULL)) wM = word_load(ws, ib + (lane * 34 + 32) * 8);
      if (nM && !(wL & FULL)) wL = word_load(ws, ib + (lane * 34 + 33) * 8);
      const bool ok = (!nA || (wA & FULL)) && (!nB || (wB & FULL)) && (!nM || ((wM & wL) & FULL));
      if (__all(ok)) break;
      __builtin_amdgcn_s_sleep(POLL_SLEEP);
    }
    STAMP(8);
    const float ms = nM ? __uint_as_float((u32)wM) : -INFINITY;
    const float lsv = nM ? __uint_as_float((u32)wL) : 0.f;
    float M = ms;
    M = fmaxf(M, dpp<XOR1>(M)); M = fmaxf(M, dpp<XOR2>(M)); M = fmaxf(M, dpp<ROR4>(M)); M = fmaxf(M, dpp<XOR8>(M)); M = x16_max(M); M = x32_max(M);
    const float wt = nM ? __builtin_amdgcn_exp2f(ms - M) : 0.f;
    float L = wt * lsv;
    L += dpp<XOR1>(L); L += dpp<XOR2>(L); L += dpp<ROR4>(L); L += dpp<XOR8>(L); L = swap16_add(L, L); L = swap32_add(L, L);
    const float cw = wt / L;
    const float cA = __shfl(cw, sA, 64), cB = __shfl(cw, sB, 64);
    float v = (nA ? cA * __uint_as_float((u32)wA) : 0.f) + (nB ? cB * __uint_as_float((u32)wB) : 0.f);
    v = swap32_add(v, v);
    if (lane < 32) lds[L_RED2 + wave * 32 + dd] = v;
  }
  lds_barrier();
  STAMP(9);
  if (t < 448) {
    float pacc[8];
#pragma unroll
    for (int k = 0; k < 8; ++k) pacc[k] = 0.f;
#pragma unroll
    for (int i = 0; i < 4; ++i) {
      const int row = rg3 + 8 * i;
      float ov = 0.f;
#pragma unroll
      for (int w = 0; w < NWAVE; ++w) ov += lds[L_RED2 + w * 32 + row];
      ov = rbf(ov);
#pragma unroll
      for (int k = 0; k < 4; ++k) { pacc[2 * k] = fmaf(ov, bflo(Wo[i][k]), pacc[2 * k]); pacc[2 * k + 1] = fmaf(ov, bfhi(Wo[i][k]), pacc[2 * k + 1]); }
    }
    float* r3 = &lds[L_RED3 + rg3 * 448 + cg3 * 8];
    *reinterpret_cast<f32x4*>(r3) = f32x4{pacc[0], pacc[1], pacc[2], pacc[3]};
    *reinterpret_cast<f32x4*>(r3 + 4) = f32x4{pacc[4], pacc[5], pacc[6], pacc[7]};
  }
  lds_barrier();
  STAMP(10);
  if (t < 448) {
    float sacc = lds[L_RED3 + t];
#pragma unroll
    for (int rg = 1; rg < 8; ++rg) sacc += lds[L_RED3 + rg * 448 + t];
    word_store(ws, WS_I2 + (t >> 2) * 8192 + (j * 4 + (t & 3)) * 8, FULL | (u64)__float_as_uint(sacc));
  }
  STAMP(11);

  if (j < 112) {
    f32x4 v = {0.f, 0.f, 0.f, 0.f};
    if (t < 256) {
      u64 w[4] = {0ull, 0ull, 0ull, 0ull};
      for (unsigned spins = 0; spins < SPIN_CAP; ++spins) {
#pragma unroll
        for (int k = 0; k < 4; ++k) if (!(w[k] & FULL)) w[k] = word_load(ws, WS_I2 + j * 8192 + (t * 4 + k) * 8);
        const bool ok = (w[0] & w[1] & w[2] & w[3] & FULL) != 0ull;
        if (__all(ok)) break;
        __builtin_amdgcn_s_sleep(POLL_SLEEP);
      }
#pragma unroll
      for (int k = 0; k < 4; ++k) {
        float q = __uint_as_float((u32)w[k]);
        q += dpp<XOR1>(q); q += dpp<XOR2>(q); q += dpp<ROR4>(q); q += dpp<XOR8>(q); q = swap16_add(q, q); q = swap32_add(q, q);
        v[k] = q;
      }
      if (lane == 0) *reinterpret_cast<f32x4*>(&lds[L_FIN + wave * 4]) = v;
    }
    lds_barrier();
    STAMP(12);
    if (t == 0) {
      const float g = lds[L_GP + 8], og = rbf(1.f - g);
      u32 o[4];
#pragma unroll
      for (int k = 0; k < 4; ++k) {
        const float a = rbf(lds[L_FIN + k] + lds[L_FIN + 4 + k]), b = rbf(lds[L_FIN + 8 + k] + lds[L_FIN + 12 + k]);
        o[k] = f2bf_bits(rbf(g * a) + rbf(og * b));
      }
      *reinterpret_cast<u32x2*>(P.out + j * 4) = u32x2{o[0] | (o[1] << 16), o[2] | (o[3] << 16)};
      if (j == 0) *reinterpret_cast<u64*>(ws + WS_EPOCH) = tag;
    }
  }
  STAMP(13);
}
}

extern "C" void kernel_launch(void* const* d_in, const int* in_sizes, int n_in, void* d_out, int out_size, void* d_ws, size_t ws_size,
                              hipStream_t stream) {
  Ptrs P;
  P.x = (const u16*)d_in[0]; P.kA = (const u16*)d_in[1]; P.vA = (const u16*)d_in[2]; P.wqA = (const u16*)d_in[3]; P.woA = (const u16*)d_in[4];
  P.kB = (const u16*)d_in[6]; P.vB = (const u16*)d_in[7]; P.wqB = (const u16*)d_in[8]; P.woB = (const u16*)d_in[9]; P.gw = (const u16*)d_in[10];
  P.out = (u16*)d_out; P.ws = (unsigned char*)d_ws;
  hipLaunchKernelGGL(fused_decode, dim3(256), dim3(NTHR), 0, stream, P);
}
```
